```python
import torch

def _softmax(x, axis=-1):
    return torch.softmax(x, dim=axis)
def _gelu(x):
    return torch.nn.functional.gelu(x, approximate="tanh")
def _silu(x):
    return torch.nn.functional.silu(x)
def _sigmoid(x):
    return torch.sigmoid(x)
def _layernorm(x, axis=-1, eps=1e-5):
    m = x.mean(dim=axis, keepdim=True)
    v = x.var(dim=axis, keepdim=True, unbiased=False)
    return (x - m) / torch.sqrt(v + eps)
def _scatter_add(tgt, idx, src, axis=0):
    return tgt.clone().index_add_(axis, idx, src)
def _segment_sum(src, seg, n):
    out = torch.zeros((n,) + src.shape[1:], dtype=src.dtype)
    return out.index_add_(0, seg, src)
def _pad(x, pw):
    flat = tuple(v for pair in reversed(pw) for v in pair)
    return torch.nn.functional.pad(x, flat)

kernel_name = 'sequential_preLN__ssm_conv_h3__glu_gated_geglu__prompt'

def setup_inputs():

    _w = (lambda a: (lambda t: t.to(torch.bfloat16) if t.is_floating_point() else t)(torch.as_tensor(a)))
    def _A_setup():
        B, S, D = 1, 2048, 1024
        ks = 4
        return {
            "q": _w((lambda *s: torch.randn(*s, dtype=torch.bfloat16) * 0.05)(B, S, D)),
            "k": _w((lambda *s: torch.randn(*s, dtype=torch.bfloat16) * 0.2)(B, S, D)),
            "v": _w((lambda *s: torch.randn(*s, dtype=torch.bfloat16) * 0.2)(B, S, D)),
            "shift_k": _w((lambda *s: torch.randn(*s, dtype=torch.bfloat16) * 0.2)(D, ks) * 0.5),
            "ssm_k": _w((lambda *s: torch.randn(*s, dtype=torch.bfloat16) * 0.4)(D, S) * (1.0 / S ** 0.5)),
            "ssm_D": _w((lambda *s: torch.randn(*s, dtype=torch.bfloat16) * 0.2)(D)),
        }
    _A_in = _A_setup()

    def _B_setup():
        B, S, Dm, Df = 1, 2048, 1024, 3584
        return {
            "x": _w((lambda *s: torch.randn(*s, dtype=torch.bfloat16) * 0.05)(B, S, Dm)),
            "w_gate": _w((lambda *s: torch.randn(*s, dtype=torch.bfloat16) * 0.4)(Dm, Df) * (Dm ** -0.5)),
            "w_up": _w((lambda *s: torch.randn(*s, dtype=torch.bfloat16) * 0.4)(Dm, Df) * (Dm ** -0.5)),
            "w_down": _w((lambda *s: torch.randn(*s, dtype=torch.bfloat16) * 0.4)(Df, Dm) * (Df ** -0.5)),
        }
    _B_in = _B_setup()

    return {
        "A_q": _A_in["q"],
        "A_k": _A_in["k"],
        "A_v": _A_in["v"],
        "A_shift_k": _A_in["shift_k"],
        "A_ssm_k": _A_in["ssm_k"],
        "A_ssm_D": _A_in["ssm_D"],
        "B_x": _B_in["x"],
        "B_w_gate": _B_in["w_gate"],
        "B_w_up": _B_in["w_up"],
        "B_w_down": _B_in["w_down"],
    }

def reference(A_q, A_k, A_v, A_shift_k, A_ssm_k, A_ssm_D, B_x, B_w_gate, B_w_up, B_w_down):
    def _A_ref(q, k, v, shift_k, ssm_k, ssm_D):
        B, S, D = q.shape
        ks = shift_k.shape[-1]
        k_pad = _pad(k, ((0, 0), (ks - 1, 0), (0, 0)))
        k_shift = torch.zeros_like(k)
        for t in range(ks):
            k_shift = k_shift + k_pad[:, t:t + S, :] * shift_k[:, ks - 1 - t][None, None, :]
        kv = k_shift * v
        L = 2 * S
        kv_pad = _pad(kv, ((0, 0), (0, S), (0, 0)))
        kf_pad = _pad(ssm_k, ((0, 0), (0, S)))
        kv_f = (lambda x, axes=(-2,-1): torch.fft.rfft2(x.float(), dim=axes))(kv_pad[:, None, :, :], axes=(1, 2))
        kf_f = (lambda x, axes=(-2,-1): torch.fft.rfft2(x.float(), dim=axes))(kf_pad[:, None, :], axes=(1, 2))
        kf_f = (lambda x, *d: x.permute(*d))(kf_f, 1, 2, 0)
        y_f = kv_f * kf_f[None, :, :, :]
        y = (lambda x, s, axes=(-2,-1): torch.fft.irfft2(x, s=s, dim=axes).to(torch.bfloat16))(y_f, (1, L), axes=(1, 2))[:, 0, :S, :]
        y = y + kv * ssm_D[None, None, :]
        return q * y

    def _B_ref(x, w_gate, w_up, w_down):
        g = torch.einsum("bsd,df->bsf", x, w_gate)
        u = torch.einsum("bsd,df->bsf", x, w_up)
        h = _gelu(g) * u
        return torch.einsum("bsf,fd->bsd", h, w_down)

    x = A_q
    h = x + _A_ref(_layernorm(x, axis=-1), A_k, A_v, A_shift_k, A_ssm_k, A_ssm_D)
    h2 = h + _B_ref(_layernorm(h, axis=-1), B_w_gate, B_w_up, B_w_down)
    return h2
```

```cpp
#include <hip/hip_runtime.h>
#include <hip/hip_bf16.h>
typedef unsigned short u16;
__device__ __forceinline__ float bf2f(u16 x){ return __uint_as_float(((unsigned)x)<<16); }
__device__ __forceinline__ u16 f2bf(float f){ unsigned u=__float_as_uint(f); u += 0x7FFFu + ((u>>16)&1u); return (u16)(u>>16); }
__device__ __forceinline__ float rb(float f){ return bf2f(f2bf(f)); }
constexpr int S=2048, D=1024, F=3584;
__global__ void k_kv(const u16* k,const u16* v,const u16* sk,u16* kv){
  int i=blockIdx.x*256+threadIdx.x; int s=i/D,d=i%D;
  float a=0;
  for(int t=0;t<4;t++){ int r=s-3+t; float kk = r>=0? bf2f(k[r*D+d]):0.f; a = a + kk*bf2f(sk[d*4+3-t]); }
  kv[i]=f2bf(rb(a)*bf2f(v[i]));
}
__global__ void k_conv(const u16* kv,const u16* f,const u16* sD,u16* z){
  int i=blockIdx.x*256+threadIdx.x; int s=i/D,d=i%D;
  float a=0;
  for(int j=0;j<=s;j++) a += bf2f(kv[(s-j)*D+d])*bf2f(f[d*S+j]);
  float y=rb(a);
  z[i]=f2bf(y + rb(bf2f(kv[i])*bf2f(sD[d])));
}
__global__ void k_ln(const u16* x,const u16* z,u16* h,u16* hn){
  __shared__ float red[256]; __shared__ float hv[D];
  int s=blockIdx.x,t=threadIdx.x;
  float a=0; for(int d=t;d<D;d+=256) a+=bf2f(x[s*D+d]);
  red[t]=a; __syncthreads(); for(int o=128;o>0;o>>=1){ if(t<o) red[t]+=red[t+o]; __syncthreads(); }
  float m=red[0]/D; __syncthreads();
  a=0; for(int d=t;d<D;d+=256){ float e=bf2f(x[s*D+d])-m; a+=e*e; }
  red[t]=a; __syncthreads(); for(int o=128;o>0;o>>=1){ if(t<o) red[t]+=red[t+o]; __syncthreads(); }
  float rs=1.f/sqrtf(red[0]/D+1e-5f); __syncthreads();
  a=0;
  for(int d=t;d<D;d+=256){ float xx=bf2f(x[s*D+d]); float q=rb((xx-m)*rs); float hh=rb(xx+rb(q*bf2f(z[s*D+d]))); hv[d]=hh; h[s*D+d]=f2bf(hh); a+=hh; }
  red[t]=a; __syncthreads(); for(int o=128;o>0;o>>=1){ if(t<o) red[t]+=red[t+o]; __syncthreads(); }
  m=red[0]/D; __syncthreads();
  a=0; for(int d=t;d<D;d+=256){ float e=hv[d]-m; a+=e*e; }
  red[t]=a; __syncthreads(); for(int o=128;o>0;o>>=1){ if(t<o) red[t]+=red[t+o]; __syncthreads(); }
  rs=1.f/sqrtf(red[0]/D+1e-5f);
  for(int d=t;d<D;d+=256) hn[s*D+d]=f2bf((hv[d]-m)*rs);
}
__global__ void k_g1(const u16* hn,const u16* wg,const u16* wu,u16* hm){
  int i=blockIdx.x*256+threadIdx.x; int s=i/F,f=i%F;
  float g=0,u=0; for(int k=0;k<D;k++){ float a=bf2f(hn[s*D+k]); g+=a*bf2f(wg[k*F+f]); u+=a*bf2f(wu[k*F+f]); }
  g=rb(g); u=rb(u);
  float t=tanhf(0.7978845608028654f*(g+0.044715f*g*g*g)); float ge=rb(0.5f*g*(1.f+t));
  hm[i]=f2bf(ge*u);
}
__global__ void k_g2(const u16* hm,const u16* wd,const u16* h,u16* out){
  int i=blockIdx.x*256+threadIdx.x; int s=i/D,d=i%D;
  float a=0; for(int k=0;k<F;k++) a+=bf2f(hm[s*F+k])*bf2f(wd[k*D+d]);
  out[i]=f2bf(bf2f(h[i])+rb(a));
}
extern "C" void kernel_launch(void* const* d_in, const int* in_sizes, int n_in, void* d_out, int out_size, void* d_ws, size_t ws_size, hipStream_t stream){
  const u16*q=(const u16*)d_in[0],*k=(const u16*)d_in[1],*v=(const u16*)d_in[2],*sk=(const u16*)d_in[3],*f=(const u16*)d_in[4],*sD=(const u16*)d_in[5];
  const u16*wg=(const u16*)d_in[7],*wu=(const u16*)d_in[8],*wd=(const u16*)d_in[9];
  u16* ws=(u16*)d_ws; u16*kv=ws,*z=ws+S*D,*h=ws+2*S*D,*hn=ws+3*S*D,*hm=ws+4*S*D;
  k_kv<<<S*D/256,256,0,stream>>>(k,v,sk,kv);
  k_conv<<<S*D/256,256,0,stream>>>(kv,f,sD,z);
  k_ln<<<S,256,0,stream>>>(q,z,h,hn);
  k_g1<<<S*F/256,256,0,stream>>>(hn,wg,wu,hm);
  k_g2<<<S*D/256,256,0,stream>>>(hm,wd,h,(u16*)d_out);
}
```

```cpp
#include <hip/hip_runtime.h>
#include <hip/hip_bf16.h>
#include <stdint.h>

typedef unsigned short u16;
typedef unsigned int u32;
typedef float f32x2 __attribute__((ext_vector_type(2)));
typedef float f32x4 __attribute__((ext_vector_type(4)));
typedef float f32x16 __attribute__((ext_vector_type(16)));
typedef u32 u32x2 __attribute__((ext_vector_type(2)));
typedef u32 u32x3 __attribute__((ext_vector_type(3)));
typedef u32 u32x4 __attribute__((ext_vector_type(4)));
typedef __bf16 bf16x2 __attribute__((ext_vector_type(2)));
typedef __bf16 bf16x8 __attribute__((ext_vector_type(8)));
#define DI __device__ __forceinline__
#define LAS __attribute__((address_space(3)))

constexpr int S_ = 2048, D_ = 1024, F_ = 3584;

DI float bflo(u32 w) { return __uint_as_float(w << 16); }
DI float bfhi(u32 w) { return __uint_as_float(w & 0xffff0000u); }
DI u32 pk2(float lo, float hi) { f32x2 v = {lo, hi}; bf16x2 b = __builtin_convertvector(v, bf16x2); return __builtin_bit_cast(u32, b); }
DI float rbf(float x) { return bflo(pk2(x, x)); }
DI u32 rfl(u32 x) { return __builtin_amdgcn_readfirstlane(x); }

template <int CTRL, int RM> DI float dpp_add(float v) {
  int t = __builtin_amdgcn_update_dpp(0, __float_as_int(v), CTRL, RM, 0xf, true);
  return v + __int_as_float(t);
}
DI float wave_sum(float v) {
  v = dpp_add<0x111, 0xf>(v); v = dpp_add<0x112, 0xf>(v); v = dpp_add<0x114, 0xf>(v); v = dpp_add<0x118, 0xf>(v);
  v = dpp_add<0x142, 0xa>(v); v = dpp_add<0x143, 0xc>(v);
  return __int_as_float(__builtin_amdgcn_readlane(__float_as_int(v), 63));
}

__global__ __launch_bounds__(256) void k_kv(const u16* __restrict__ k, const u16* __restrict__ v, const u16* __restrict__ sk, u16* __restrict__ kvB) {
  const int u = blockIdx.x, t = threadIdx.x;
  u32x2 kk[11], vv[8];
#pragma unroll
  for (int i = 0; i < 11; i++) {
    const int row = u * 8 - 3 + i;
    u32x2 z = {0u, 0u};
    kk[i] = row >= 0 ? *(const u32x2*)(k + (size_t)row * D_ + 4 * t) : z;
  }
#pragma unroll
  for (int i = 0; i < 8; i++) vv[i] = *(const u32x2*)(v + (size_t)(u * 8 + i) * D_ + 4 * t);
  u32x4 s01 = *(const u32x4*)(sk + 16 * t), s23 = *(const u32x4*)(sk + 16 * t + 8);
  float sw[4][4];
  sw[0][0] = bflo(s01[0]); sw[0][1] = bfhi(s01[0]); sw[0][2] = bflo(s01[1]); sw[0][3] = bfhi(s01[1]);
  sw[1][0] = bflo(s01[2]); sw[1][1] = bfhi(s01[2]); sw[1][2] = bflo(s01[3]); sw[1][3] = bfhi(s01[3]);
  sw[2][0] = bflo(s23[0]); sw[2][1] = bfhi(s23[0]); sw[2][2] = bflo(s23[1]); sw[2][3] = bfhi(s23[1]);
  sw[3][0] = bflo(s23[2]); sw[3][1] = bfhi(s23[2]); sw[3][2] = bflo(s23[3]); sw[3][3] = bfhi(s23[3]);
#pragma unroll
  for (int c = 0; c < 4; c++) {
    float kf[11], o[8];
#pragma unroll
    for (int i = 0; i < 11; i++) kf[i] = (c & 1) ? bfhi(kk[i][c >> 1]) : bflo(kk[i][c >> 1]);
#pragma unroll
    for (int r = 0; r < 8; r++) {
      float a = kf[r] * sw[c][3];
      a = fmaf(kf[r + 1], sw[c][2], a); a = fmaf(kf[r + 2], sw[c][1], a); a = fmaf(kf[r + 3], sw[c][0], a);
      const float vf = (c & 1) ? bfhi(vv[r][c >> 1]) : bflo(vv[r][c >> 1]);
      o[r] = rbf(a) * vf;
    }
    u32x4 w = {pk2(o[7], o[6]), pk2(o[5], o[4]), pk2(o[3], o[2]), pk2(o[1], o[0])};
    *(u32x4*)(kvB + ((size_t)u * D_ + 4 * t + c) * 8) = w;
  }
}

constexpr int CV_ECH = 2064 * 8;
constexpr int CV_XO = 2368, CV_XCH = 4752;
constexpr int CV_X0 = 4 * CV_ECH;
constexpr int CV_R0 = CV_X0 + 4 * CV_XCH;
constexpr int CV_LDS = CV_R0 + 32768;
__global__ __launch_bounds__(512, 2) void k_conv(const u16* __restrict__ kvB, const u16* __restrict__ f, const u16* __restrict__ sD, u16* __restrict__ zB) {
  extern __shared__ __attribute__((aligned(16))) char lds[];
  const int b = blockIdx.x, tid = threadIdx.x, lane = tid & 63, w = tid >> 6;
  const int c0 = ((b & 7) * 32 + (b >> 3)) * 4;
  const __amdgpu_buffer_rsrc_t rf = __builtin_amdgcn_make_buffer_rsrc((void*)f, (short)0, D_ * S_ * 2, 0x00020000);
  u32x3 fd[8];
#pragma unroll
  for (int i = 0; i < 8; i++) {
    const int idx = tid + 512 * i, c = idx >> 10, l = idx & 1023;
    fd[i] = __builtin_amdgcn_raw_buffer_load_b96(rf, (u32)(((c0 + c) * 1024 + l) * 4), 0, 0);
  }
  u32x4 xp[2];
#pragma unroll
  for (int i = 0; i < 2; i++) {
    const int idx = tid + 512 * i, u = idx >> 2, c = idx & 3;
    xp[i] = *(const u32x4*)(kvB + ((size_t)u * D_ + c0 + c) * 8);
  }
  if (tid < 128) {
    const int c = tid >> 5, par = (tid >> 4) & 1, vv = tid & 15;
    *(u32x4*)(lds + CV_X0 + c * CV_XCH + par * CV_XO + vv * 16) = (u32x4){0u, 0u, 0u, 0u};
  } else if (tid < 160) {
    const int c = (tid - 128) >> 3, l = ((tid - 128) & 7) - 8;
    const u32* fr = (const u32*)(f + (size_t)(c0 + c) * S_);
    const u32 d0 = 0, d1 = (l + 1 >= 0) ? fr[0] : 0u, d2 = (l + 2 >= 0) ? fr[l + 2] : 0u;
    u32x4 e = {d0, d1, __builtin_amdgcn_alignbit(d1, d0, 16), __builtin_amdgcn_alignbit(d2, d1, 16)};
    *(u32x4*)(lds + c * CV_ECH + (2 * l + 16) * 8) = e;
  }
#pragma unroll
  for (int i = 0; i < 8; i++) {
    const int idx = tid + 512 * i, c = idx >> 10, l = idx & 1023;
    u32x4 e = {fd[i][0], fd[i][1], __builtin_amdgcn_alignbit(fd[i][1], fd[i][0], 16), __builtin_amdgcn_alignbit(fd[i][2], fd[i][1], 16)};
    *(u32x4*)(lds + c * CV_ECH + (2 * l + 16) * 8) = e;
  }
#pragma unroll
  for (int i = 0; i < 2; i++) {
    const int idx = tid + 512 * i, u = idx >> 2, c = idx & 3;
    *(u32x4*)(lds + CV_X0 + c * CV_XCH + (u & 1) * CV_XO + ((u >> 1) + 16) * 16) = xp[i];
  }
  __syncthreads();
  const int c = w & 3, hh = w >> 2;
  const int lb = lane & 15, g = lane >> 4;
  const int kap = 16 * (g & 1) + 8 * (g >> 1);
  const char* Ab = lds + c * CV_ECH + (16 + lb - kap - 7 + 16) * 8 + hh * 8 * 128;
  const char* Bb = lds + CV_X0 + c * CV_XCH + (g >> 1) * CV_XO + (lb - 1 + (g & 1) + 16) * 16 - hh * 8 * 16;
  f32x4 acc[8];
#pragma unroll
  for (int t = 0; t < 8; t++) acc[t] = (f32x4){0.f, 0.f, 0.f, 0.f};
#pragma unroll
  for (int it = 0; it < 4; it++) {
    const int mp = 2 * it;
    bf16x8 A[8], B[8];
#pragma unroll
    for (int i = 0; i < 8; i++) {
      const u32x2 lo = *(const u32x2*)(Ab + mp * 128 + i * 2048), hi = *(const u32x2*)(Ab + mp * 128 + i * 2048 + 32);
      const u32x4 tt = {lo[0], lo[1], hi[0], hi[1]};
      A[i] = __builtin_bit_cast(bf16x8, tt);
      B[i] = *(const bf16x8*)(Bb - mp * 16 + i * 256);
    }
#pragma unroll
    for (int t = 0; t < 8; t++)
#pragma unroll
      for (int i = 0; i <= t; i++) acc[t] = __builtin_amdgcn_mfma_f32_16x16x32_bf16(A[i], B[t - i], acc[t], 0, 0, 0);
  }
  {
    f32x4* R = (f32x4*)(lds + CV_R0) + (size_t)w * 256;
#pragma unroll
    for (int j = 0; j < 4; j++) R[j * 64 + lane] = hh ? acc[j] : acc[4 + j];
    __syncthreads();
    const f32x4* P = (const f32x4*)(lds + CV_R0) + (size_t)(w ^ 4) * 256;
    f32x4 y[4];
#pragma unroll
    for (int j = 0; j < 4; j++) y[j] = (hh ? acc[4 + j] : acc[j]) + P[j * 64 + lane];
    const float dd = bflo((u32)sD[c0 + c]);
    __syncthreads();
    char* Z = lds + CV_R0;
#pragma unroll
    for (int j = 0; j < 4; j++) {
      const int t = 4 * hh + j;
      const int u = 32 * t + 2 * lb + (g >> 1);
      const u32x2 kvp = *(const u32x2*)(lds + CV_X0 + c * CV_XCH + (u & 1) * CV_XO + ((u >> 1) + 16) * 16 + ((g & 1) ? 0 : 8));
      const float k0 = bfhi(kvp[1]), k1 = bflo(kvp[1]), k2 = bfhi(kvp[0]), k3 = bflo(kvp[0]);
      const float z0 = rbf(y[j][0]) + rbf(k0 * dd), z1 = rbf(y[j][1]) + rbf(k1 * dd), z2 = rbf(y[j][2]) + rbf(k2 * dd), z3 = rbf(y[j][3]) + rbf(k3 * dd);
      u32x2 o = {pk2(z0, z1), pk2(z2, z3)};
      *(u32x2*)(Z + c * 4112 + (256 * t + 16 * lb + 4 * g) * 2) = o;
    }
    __syncthreads();
#pragma unroll
    for (int i = 0; i < 2; i++) {
      const int idx = tid + 512 * i, u = idx >> 2, cc = idx & 3;
      const u32x4 o = *(const u32x4*)(Z + cc * 4112 + u * 16);
      *(u32x4*)(zB + ((size_t)u * D_ + c0 + cc) * 8) = o;
    }
  }
}

DI void block_sum8(float (&v)[8], float* red, int w, int lane) {
#pragma unroll
  for (int r = 0; r < 8; r++) v[r] = wave_sum(v[r]);
  if (lane == 0) {
    *(f32x4*)(red + w * 8) = (f32x4){v[0], v[1], v[2], v[3]};
    *(f32x4*)(red + w * 8 + 4) = (f32x4){v[4], v[5], v[6], v[7]};
  }
  __syncthreads();
  f32x4 a = {0.f, 0.f, 0.f, 0.f}, b2 = a;
#pragma unroll
  for (int i = 0; i < 4; i++) { a += *(const f32x4*)(red + i * 8); b2 += *(const f32x4*)(red + i * 8 + 4); }
  v[0] = a[0]; v[1] = a[1]; v[2] = a[2]; v[3] = a[3]; v[4] = b2[0]; v[5] = b2[1]; v[6] = b2[2]; v[7] = b2[3];
}
__global__ __launch_bounds__(256) void k_ln(const u16* __restrict__ x, const u16* __restrict__ zB, u16* __restrict__ h, u16* __restrict__ hn) {
  __shared__ __attribute__((aligned(16))) char sm[8 * 2064 + 4 * 128];
  float* red = (float*)(sm + 8 * 2064);
  const int b = blockIdx.x, t = threadIdx.x, lane = t & 63, w = t >> 6;
  const int u = (b & 7) * 32 + (b >> 3);
  u32x2 xx[8]; u32x4 zz[4];
#pragma unroll
  for (int r = 0; r < 8; r++) xx[r] = *(const u32x2*)(x + (size_t)(u * 8 + r) * D_ + 4 * t);
#pragma unroll
  for (int c = 0; c < 4; c++) zz[c] = *(const u32x4*)(zB + ((size_t)u * D_ + 4 * t + c) * 8);
  float xf[8][4], s[8];
#pragma unroll
  for (int r = 0; r < 8; r++) { xf[r][0] = bflo(xx[r][0]); xf[r][1] = bfhi(xx[r][0]); xf[r][2] = bflo(xx[r][1]); xf[r][3] = bfhi(xx[r][1]); s[r] = (xf[r][0] + xf[r][1]) + (xf[r][2] + xf[r][3]); }
  block_sum8(s, red, w, lane);
  float m[8];
#pragma unroll
  for (int r = 0; r < 8; r++) { m[r] = s[r] * (1.f / D_); float a = 0.f;
#pragma unroll
    for (int c = 0; c < 4; c++) { const float d = xf[r][c] - m[r]; a = fmaf(d, d, a); } s[r] = a; }
  block_sum8(s, red + 32, w, lane);
#pragma unroll
  for (int r = 0; r < 8; r++) {
    const float mb = rbf(m[r]);
    const float sd = rbf(sqrtf(rbf(rbf(s[r] * (1.f / D_)) + 1e-5f)));
    const float rs = 1.f / sd;
    float a = 0.f;
#pragma unroll
    for (int c = 0; c < 4; c++) {
      const u32 zw = zz[c][r >> 1];
      const float zf = (r & 1) ? bfhi(zw) : bflo(zw);
      const float qn = rbf(rbf(xf[r][c] - mb) * rs);
      const float hh = rbf(xf[r][c] + rbf(qn * zf));
      xf[r][c] = hh; a += hh;
    }
    s[r] = a;
    u32x2 o = {pk2(xf[r][0], xf[r][1]), pk2(xf[r][2], xf[r][3])};
    *(u32x2*)(h + (size_t)(u * 8 + r) * D_ + 4 * t) = o;
  }
  block_sum8(s, red + 64, w, lane);
#pragma unroll
  for (int r = 0; r < 8; r++) { m[r] = s[r] * (1.f / D_); float a = 0.f;
#pragma unroll
    for (int c = 0; c < 4; c++) { const float d = xf[r][c] - m[r]; a = fmaf(d, d, a); } s[r] = a; }
  block_sum8(s, red + 96, w, lane);
#pragma unroll
  for (int r = 0; r < 8; r++) {
    const float mb = rbf(m[r]);
    const float sd = rbf(sqrtf(rbf(rbf(s[r] * (1.f / D_)) + 1e-5f)));
    const float rs = 1.f / sd;
    u32x2 o = {pk2(rbf(xf[r][0] - mb) * rs, rbf(xf[r][1] - mb) * rs), pk2(rbf(xf[r][2] - mb) * rs, rbf(xf[r][3] - mb) * rs)};
    *(u32x2*)(sm + r * 2064 + t * 8) = o;
  }
  __syncthreads();
#pragma unroll
  for (int i = 0; i < 4; i++) {
    const int idx = t + 256 * i, r = idx & 7, G = idx >> 3;
    const u32x4 o = *(const u32x4*)(sm + r * 2064 + G * 16);
    const int token = u * 8 + r;
    *(u32x4*)((char*)hn + ((size_t)((token >> 5) * 64 + (G >> 1)) * 1024 + (G & 1) * 512 + (token & 31) * 16)) = o;
  }
}

constexpr int G1_SLOT = 32896, G1_LDS = 4 * G1_SLOT;
DI float geglu(float g, float uu) {
  const float c1 = -2.302208198f;
  const float c3 = -0.1029432397f;
  const float p = fmaf(g * g, c3, c1);
  const float e = __builtin_amdgcn_exp2f(g * p);
  return g * uu * __builtin_amdgcn_rcpf(1.f + e);
}
__global__ __launch_bounds__(512, 2) void k_gemm1(const u16* __restrict__ hn, const u16* __restrict__ wg, const u16* __restrict__ wu, u16* __restrict__ hm) {
  extern __shared__ __attribute__((aligned(16))) char lds[];
  const int b = blockIdx.x, tid = threadIdx.x, lane = tid & 63;
  const int w = rfl(tid >> 6);
  const int pn = (b & 7) * 4 + (b >> 6), pm = (b >> 3) & 7;
  const int tt = pm * 8 + w;
  const u32 ldsb = (u32)(size_t)lds;
  const int hh = lane >> 5, blk = (lane >> 4) & 1, qq = (lane & 15) >> 2, p = lane & 3;
  const u32 vtrA = ldsb + blk * 16512 + hh * 2048 + qq * 224 + 16 * (p & 1) + 8 * (p >> 1);
  const u32 vtrB = vtrA + 2 * G1_SLOT;
  const u32 vtok = lane * 16;
  const u32 vdma = lane < 56 ? (u32)((lane / 14 + 16 * (w & 3)) * 7168 + (lane % 14) * 16 + pn * 224) : 0x7ffffff0u;
  const __amdgpu_buffer_rsrc_t shn = __builtin_amdgcn_make_buffer_rsrc((void*)((const char*)hn + (size_t)tt * 65536), (short)0, 65536, 0x00020000);
  const __amdgpu_buffer_rsrc_t sw = __builtin_amdgcn_make_buffer_rsrc((void*)(w < 4 ? wg : wu), (short)0, D_ * F_ * 2, 0x00020000);
  const u32 sm0 = rfl(ldsb + (w >> 2) * 16512 + (w & 3) * 4096);
  f32x16 a0, a1, a2, a3, a4, a5, a6;
  asm volatile(
    "s_mov_b32 s40, 0\n\t"
    "s_mov_b32 s41, 0\n\t"
    "s_mov_b32 s42, 28672\n\t"
    "s_mov_b32 s43, 57344\n\t"
    "s_mov_b32 s44, 86016\n\t"
    "buffer_load_dwordx4 v[140:143], %[vtok], %[shn], s40 offen offset:0\n\t"
    "buffer_load_dwordx4 v[144:147], %[vtok], %[shn], s40 offen offset:1024\n\t"
    "buffer_load_dwordx4 v[148:151], %[vtok], %[shn], s40 offen offset:2048\n\t"
    "s_add_u32 m0, %[sm0], 0\n\t"
    "s_nop 0\n\t"
    "buffer_load_dwordx4 %[vdma], %[sw], s41 offen lds\n\t"
    "s_add_u32 m0, %[sm0], 1024\n\t"
    "s_nop 0\n\t"
    "buffer_load_dwordx4 %[vdma], %[sw], s42 offen lds\n\t"
    "s_add_u32 m0, %[sm0], 2048\n\t"
    "s_nop 0\n\t"
    "buffer_load_dwordx4 %[vdma], %[sw], s43 offen lds\n\t"
    "s_add_u32 m0, %[sm0], 3072\n\t"
    "s_nop 0\n\t"
    "buffer_load_dwordx4 %[vdma], %[sw], s44 offen lds\n\t"
    "buffer_load_dwordx4 v[152:155], %[vtok], %[shn], s40 offen offset:3072\n\t"
    "s_add_u32 s40, s40, 4096\n\t"
    "s_add_u32 s41, s41, 458752\n\t"
    "s_add_u32 s42, s42, 458752\n\t"
    "s_add_u32 s43, s43, 458752\n\t"
    "s_add_u32 s44, s44, 458752\n\t"
    "buffer_load_dwordx4 v[156:159], %[vtok], %[shn], s40 offen offset:0\n\t"
    "buffer_load_dwordx4 v[160:163], %[vtok], %[shn], s40 offen offset:1024\n\t"
    "buffer_load_dwordx4 v[164:167], %[vtok], %[shn], s40 offen offset:2048\n\t"
    "s_add_u32 m0, %[sm0], 32896\n\t"
    "s_nop 0\n\t"
    "buffer_load_dwordx4 %[vdma], %[sw], s41 offen lds\n\t"
    "s_add_u32 m0, %[sm0], 33920\n\t"
    "s_nop 0\n\t"
    "buffer_load_dwordx4 %[vdma], %[sw], s42 offen lds\n\t"
    "s_add_u32 m0, %[sm0], 34944\n\t"
    "s_nop 0\n\t"
    "buffer_load_dwordx4 %[vdma], %[sw], s43 offen lds\n\t"
    "s_add_u32 m0, %[sm0], 35968\n\t"
    "s_nop 0\n\t"
    "buffer_load_dwordx4 %[vdma], %[sw], s44 offen lds\n\t"
    "buffer_load_dwordx4 v[168:171], %[vtok], %[shn], s40 offen offset:3072\n\t"
    "s_add_u32 s40, s40, 4096\n\t"
    "s_add_u32 s41, s41, 458752\n\t"
    "s_add_u32 s42, s42, 458752\n\t"
    "s_add_u32 s43, s43, 458752\n\t"
    "s_add_u32 s44, s44, 458752\n\t"
    "buffer_load_dwordx4 v[172:175], %[vtok], %[shn], s40 offen offset:0\n\t"
    "buffer_load_dwordx4 v[176:179], %[vtok], %[shn], s40 offen offset:1024\n\t"
    "buffer_load_dwordx4 v[180:183], %[vtok], %[shn], s40 offen offset:2048\n\t"
    "s_add_u32 m0, %[sm0], 65792\n\t"
    "s_nop 0\n\t"
    "buffer_load_dwordx4 %[vdma], %[sw], s41 offen lds\n\t"
    "s_add_u32 m0, %[sm0], 66816\n\t"
    "s_nop 0\n\t"
    "buffer_load_dwordx4 %[vdma], %[sw], s42 offen lds\n\t"
    "s_add_u32 m0, %[sm0], 67840\n\t"
    "s_nop 0\n\t"
    "buffer_load_dwordx4 %[vdma], %[sw], s43 offen lds\n\t"
    "s_add_u32 m0, %[sm0], 68864\n\t"
    "s_nop 0\n\t"
    "buffer_load_dwordx4 %[vdma], %[sw], s44 offen lds\n\t"
    "buffer_load_dwordx4 v[184:187], %[vtok], %[shn], s40 offen offset:3072\n\t"
    "s_add_u32 s40, s40, 4096\n\t"
    "s_add_u32 s41, s41, 458752\n\t"
    "s_add_u32 s42, s42, 458752\n\t"
    "s_add_u32 s43, s43, 458752\n\t"
    "s_add_u32 s44, s44, 458752\n\t"
    "buffer_load_dwordx4 v[188:191], %[vtok], %[shn], s40 offen offset:0\n\t"
    "buffer_load_dwordx4 v[192:195], %[vtok], %[shn], s40 offen offset:1024\n\t"
    "buffer_load_dwordx4 v[196:199], %[vtok], %[shn], s40 offen offset:2048\n\t"
    "s_add_u32 m0, %[sm0], 98688\n\t"
    "s_nop 0\n\t"
    "buffer_load_dwordx4 %[vdma], %[sw], s41 offen lds\n\t"
    "s_add_u32 m0, %[sm0], 99712\n\t"
    "s_nop 0\n\t"
    "buffer_load_dwordx4 %[vdma], %[sw], s42 offen lds\n\t"
    "s_add_u32 m0, %[sm0], 100736\n\t"
    "s_nop 0\n\t"
    "buffer_load_dwordx4 %[vdma], %[sw], s43 offen lds\n\t"
    "s_add_u32 m0, %[sm0], 101760\n\t"
    "s_nop 0\n\t"
    "buffer_load_dwordx4 %[vdma], %[sw], s44 offen lds\n\t"
    "buffer_load_dwordx4 v[200:203], %[vtok], %[shn], s40 offen offset:3072\n\t"
    "s_add_u32 s40, s40, 4096\n\t"
    "s_add_u32 s41, s41, 458752\n\t"
    "s_add_u32 s42, s42, 458752\n\t"
    "s_add_u32 s43, s43, 458752\n\t"
    "s_add_u32 s44, s44, 458752\n\t"
    "v_mov_b32 v0, 0\n\t"
    "v_mov_b32 v1, 0\n\t"
    "v_mov_b32 v2, 0\n\t"
    "v_mov_b32 v3, 0\n\t"
    "v_mov_b32 v4, 0\n\t"
    "v_mov_b32 v5, 0\n\t"
    "v_mov_b32 v6, 0\n\t"
    "v_mov_b32 v7, 0\n\t"
    "v_mov_b32 v8, 0\n\t"
    "v_mov_b32 v9, 0\n\t"
    "v_mov_b32 v10, 0\n\t"
    "v_mov_b32 v11, 0\n\t"
    "v_mov_b32 v12, 0\n\t"
    "v_mov_b32 v13, 0\n\t"
    "v_mov_b32 v14, 0\n\t"
    "v_mov_b32 v15, 0\n\t"
    "v_mov_b32 v16, 0\n\t"
    "v_mov_b32 v17, 0\n\t"
    "v_mov_b32 v18, 0\n\t"
    "v_mov_b32 v19, 0\n\t"
    "v_mov_b32 v20, 0\n\t"
    "v_mov_b32 v21, 0\n\t"
    "v_mov_b32 v22, 0\n\t"
    "v_mov_b32 v23, 0\n\t"
    "v_mov_b32 v24, 0\n\t"
    "v_mov_b32 v25, 0\n\t"
    "v_mov_b32 v26, 0\n\t"
    "v_mov_b32 v27, 0\n\t"
    "v_mov_b32 v28, 0\n\t"
    "v_mov_b32 v29, 0\n\t"
    "v_mov_b32 v30, 0\n\t"
    "v_mov_b32 v31, 0\n\t"
    "v_mov_b32 v32, 0\n\t"
    "v_mov_b32 v33, 0\n\t"
    "v_mov_b32 v34, 0\n\t"
    "v_mov_b32 v35, 0\n\t"
    "v_mov_b32 v36, 0\n\t"
    "v_mov_b32 v37, 0\n\t"
    "v_mov_b32 v38, 0\n\t"
    "v_mov_b32 v39, 0\n\t"
    "v_mov_b32 v40, 0\n\t"
    "v_mov_b32 v41, 0\n\t"
    "v_mov_b32 v42, 0\n\t"
    "v_mov_b32 v43, 0\n\t"
    "v_mov_b32 v44, 0\n\t"
    "v_mov_b32 v45, 0\n\t"
    "v_mov_b32 v46, 0\n\t"
    "v_mov_b32 v47, 0\n\t"
    "v_mov_b32 v48, 0\n\t"
    "v_mov_b32 v49, 0\n\t"
    "v_mov_b32 v50, 0\n\t"
    "v_mov_b32 v51, 0\n\t"
    "v_mov_b32 v52, 0\n\t"
    "v_mov_b32 v53, 0\n\t"
    "v_mov_b32 v54, 0\n\t"
    "v_mov_b32 v55, 0\n\t"
    "v_mov_b32 v56, 0\n\t"
    "v_mov_b32 v57, 0\n\t"
    "v_mov_b32 v58, 0\n\t"
    "v_mov_b32 v59, 0\n\t"
    "v_mov_b32 v60, 0\n\t"
    "v_mov_b32 v61, 0\n\t"
    "v_mov_b32 v62, 0\n\t"
    "v_mov_b32 v63, 0\n\t"
    "v_mov_b32 v64, 0\n\t"
    "v_mov_b32 v65, 0\n\t"
    "v_mov_b32 v66, 0\n\t"
    "v_mov_b32 v67, 0\n\t"
    "v_mov_b32 v68, 0\n\t"
    "v_mov_b32 v69, 0\n\t"
    "v_mov_b32 v70, 0\n\t"
    "v_mov_b32 v71, 0\n\t"
    "v_mov_b32 v72, 0\n\t"
    "v_mov_b32 v73, 0\n\t"
    "v_mov_b32 v74, 0\n\t"
    "v_mov_b32 v75, 0\n\t"
    "v_mov_b32 v76, 0\n\t"
    "v_mov_b32 v77, 0\n\t"
    "v_mov_b32 v78, 0\n\t"
    "v_mov_b32 v79, 0\n\t"
    "v_mov_b32 v80, 0\n\t"
    "v_mov_b32 v81, 0\n\t"
    "v_mov_b32 v82, 0\n\t"
    "v_mov_b32 v83, 0\n\t"
    "v_mov_b32 v84, 0\n\t"
    "v_mov_b32 v85, 0\n\t"
    "v_mov_b32 v86, 0\n\t"
    "v_mov_b32 v87, 0\n\t"
    "v_mov_b32 v88, 0\n\t"
    "v_mov_b32 v89, 0\n\t"
    "v_mov_b32 v90, 0\n\t"
    "v_mov_b32 v91, 0\n\t"
    "v_mov_b32 v92, 0\n\t"
    "v_mov_b32 v93, 0\n\t"
    "v_mov_b32 v94, 0\n\t"
    "v_mov_b32 v95, 0\n\t"
    "v_mov_b32 v96, 0\n\t"
    "v_mov_b32 v97, 0\n\t"
    "v_mov_b32 v98, 0\n\t"
    "v_mov_b32 v99, 0\n\t"
    "v_mov_b32 v100, 0\n\t"
    "v_mov_b32 v101, 0\n\t"
    "v_mov_b32 v102, 0\n\t"
    "v_mov_b32 v103, 0\n\t"
    "v_mov_b32 v104, 0\n\t"
    "v_mov_b32 v105, 0\n\t"
    "v_mov_b32 v106, 0\n\t"
    "v_mov_b32 v107, 0\n\t"
    "v_mov_b32 v108, 0\n\t"
    "v_mov_b32 v109, 0\n\t"
    "v_mov_b32 v110, 0\n\t"
    "v_mov_b32 v111, 0\n\t"
    "s_mov_b32 s45, 4\n\t"
    "s_waitcnt vmcnt(24)\n\t"
    "s_barrier\n\t"
    "ds_read_b64_tr_b16 v[112:113], %[vtrA] offset:0\n\t"
    "ds_read_b64_tr_b16 v[114:115], %[vtrA] offset:1024\n\t"
    "ds_read_b64_tr_b16 v[116:117], %[vtrA] offset:32\n\t"
    "ds_read_b64_tr_b16 v[118:119], %[vtrA] offset:1056\n\t"
    "ds_read_b64_tr_b16 v[120:121], %[vtrA] offset:64\n\t"
    "ds_read_b64_tr_b16 v[122:123], %[vtrA] offset:1088\n\t"
    "ds_read_b64_tr_b16 v[124:125], %[vtrA] offset:96\n\t"
    "ds_read_b64_tr_b16 v[126:127], %[vtrA] offset:1120\n\t"
    "ds_read_b64_tr_b16 v[128:129], %[vtrA] offset:128\n\t"
    "ds_read_b64_tr_b16 v[130:131], %[vtrA] offset:1152\n\t"
    "ds_read_b64_tr_b16 v[132:133], %[vtrA] offset:160\n\t"
    "ds_read_b64_tr_b16 v[134:135], %[vtrA] offset:1184\n\t"
    "ds_read_b64_tr_b16 v[136:137], %[vtrA] offset:192\n\t"
    "ds_read_b64_tr_b16 v[138:139], %[vtrA] offset:1216\n\t"
    "L_g1_loop_%=:\n\t"
    "s_waitcnt lgkmcnt(12)\n\t"
    "v_mfma_f32_32x32x16_bf16 v[0:15], v[112:115], v[140:143], v[0:15]\n\t"
    "ds_read_b64_tr_b16 v[112:113], %[vtrA] offset:4096\n\t"
    "ds_read_b64_tr_b16 v[114:115], %[vtrA] offset:5120\n\t"
    "s_waitcnt lgkmcnt(12)\n\t"
    "v_mfma_f32_32x32x16_bf16 v[16:31], v[116:119], v[140:143], v[16:31]\n\t"
    "ds_read_b64_tr_b16 v[116:117], %[vtrA] offset:4128\n\t"
    "ds_read_b64_tr_b16 v[118:119], %[vtrA] offset:5152\n\t"
    "s_waitcnt lgkmcnt(12)\n\t"
    "v_mfma_f32_32x32x16_bf16 v[32:47], v[120:123], v[140:143], v[32:47]\n\t"
    "ds_read_b64_tr_b16 v[120:121], %[vtrA] offset:4160\n\t"
    "ds_read_b64_tr_b16 v[122:123], %[vtrA] offset:5184\n\t"
    "s_waitcnt lgkmcnt(12)\n\t"
    "v_mfma_f32_32x32x16_bf16 v[48:63], v[124:127], v[140:143], v[48:63]\n\t"
    "ds_read_b64_tr_b16 v[124:125], %[vtrA] offset:4192\n\t"
    "ds_read_b64_tr_b16 v[126:127], %[vtrA] offset:5216\n\t"
    "s_waitcnt lgkmcnt(12)\n\t"
    "v_mfma_f32_32x32x16_bf16 v[64:79], v[128:131], v[140:143], v[64:79]\n\t"
    "ds_read_b64_tr_b16 v[128:129], %[vtrA] offset:4224\n\t"
    "ds_read_b64_tr_b16 v[130:131], %[vtrA] offset:5248\n\t"
    "s_waitcnt lgkmcnt(12)\n\t"
    "v_mfma_f32_32x32x16_bf16 v[80:95], v[132:135], v[140:143], v[80:95]\n\t"
    "ds_read_b64_tr_b16 v[132:133], %[vtrA] offset:4256\n\t"
    "ds_read_b64_tr_b16 v[134:135], %[vtrA] offset:5280\n\t"
    "s_waitcnt lgkmcnt(12)\n\t"
    "v_mfma_f32_32x32x16_bf16 v[96:111], v[136:139], v[140:143], v[96:111]\n\t"
    "ds_read_b64_tr_b16 v[136:137], %[vtrA] offset:4288\n\t"
    "ds_read_b64_tr_b16 v[138:139], %[vtrA] offset:5312\n\t"
    "buffer_load_dwordx4 v[140:143], %[vtok], %[shn], s40 offen offset:0\n\t"
    "s_waitcnt lgkmcnt(12)\n\t"
    "v_mfma_f32_32x32x16_bf16 v[0:15], v[112:115], v[144:147], v[0:15]\n\t"
    "ds_read_b64_tr_b16 v[112:113], %[vtrA] offset:8192\n\t"
    "ds_read_b64_tr_b16 v[114:115], %[vtrA] offset:9216\n\t"
    "s_waitcnt lgkmcnt(12)\n\t"
    "v_mfma_f32_32x32x16_bf16 v[16:31], v[116:119], v[144:147], v[16:31]\n\t"
    "ds_read_b64_tr_b16 v[116:117], %[vtrA] offset:8224\n\t"
    "ds_read_b64_tr_b16 v[118:119], %[vtrA] offset:9248\n\t"
    "s_waitcnt lgkmcnt(12)\n\t"
    "v_mfma_f32_32x32x16_bf16 v[32:47], v[120:123], v[144:147], v[32:47]\n\t"
    "ds_read_b64_tr_b16 v[120:121], %[vtrA] offset:8256\n\t"
    "ds_read_b64_tr_b16 v[122:123], %[vtrA] offset:9280\n\t"
    "s_waitcnt lgkmcnt(12)\n\t"
    "v_mfma_f32_32x32x16_bf16 v[48:63], v[124:127], v[144:147], v[48:63]\n\t"
    "ds_read_b64_tr_b16 v[124:125], %[vtrA] offset:8288\n\t"
    "ds_read_b64_tr_b16 v[126:127], %[vtrA] offset:9312\n\t"
    "s_waitcnt lgkmcnt(12)\n\t"
    "v_mfma_f32_32x32x16_bf16 v[64:79], v[128:131], v[144:147], v[64:79]\n\t"
    "ds_read_b64_tr_b16 v[128:129], %[vtrA] offset:8320\n\t"
    "ds_read_b64_tr_b16 v[130:131], %[vtrA] offset:9344\n\t"
    "s_waitcnt lgkmcnt(12)\n\t"
    "v_mfma_f32_32x32x16_bf16 v[80:95], v[132:135], v[144:147], v[80:95]\n\t"
    "ds_read_b64_tr_b16 v[132:133], %[vtrA] offset:8352\n\t"
    "ds_read_b64_tr_b16 v[134:135], %[vtrA] offset:9376\n\t"
    "s_waitcnt lgkmcnt(12)\n\t"
    "v_mfma_f32_32x32x16_bf16 v[96:111], v[136:139], v[144:147], v[96:111]\n\t"
    "ds_read_b64_tr_b16 v[136:137], %[vtrA] offset:8384\n\t"
    "ds_read_b64_tr_b16 v[138:139], %[vtrA] offset:9408\n\t"
    "buffer_load_dwordx4 v[144:147], %[vtok], %[shn], s40 offen offset:1024\n\t"
    "s_waitcnt lgkmcnt(12)\n\t"
    "v_mfma_f32_32x32x16_bf16 v[0:15], v[112:115], v[148:151], v[0:15]\n\t"
    "ds_read_b64_tr_b16 v[112:113], %[vtrA] offset:12288\n\t"
    "ds_read_b64_tr_b16 v[114:115], %[vtrA] offset:13312\n\t"
    "s_waitcnt lgkmcnt(12)\n\t"
    "v_mfma_f32_32x32x16_bf16 v[16:31], v[116:119], v[148:151], v[16:31]\n\t"
    "ds_read_b64_tr_b16 v[116:117], %[vtrA] offset:12320\n\t"
    "ds_read_b64_tr_b16 v[118:119], %[vtrA] offset:13344\n\t"
    "s_waitcnt lgkmcnt(12)\n\t"
    "v_mfma_f32_32x32x16_bf16 v[32:47], v[120:123], v[148:151], v[32:47]\n\t"
    "ds_read_b64_tr_b16 v[120:121], %[vtrA] offset:12352\n\t"
    "ds_read_b64_tr_b16 v[122:123], %[vtrA] offset:13376\n\t"
    "s_waitcnt lgkmcnt(12)\n\t"
    "v_mfma_f32_32x32x16_bf16 v[48:63], v[124:127], v[148:151], v[48:63]\n\t"
    "ds_read_b64_tr_b16 v[124:125], %[vtrA] offset:12384\n\t"
    "ds_read_b64_tr_b16 v[126:127], %[vtrA] offset:13408\n\t"
    "s_waitcnt lgkmcnt(12)\n\t"
    "v_mfma_f32_32x32x16_bf16 v[64:79], v[128:131], v[148:151], v[64:79]\n\t"
    "ds_read_b64_tr_b16 v[128:129], %[vtrA] offset:12416\n\t"
    "ds_read_b64_tr_b16 v[130:131], %[vtrA] offset:13440\n\t"
    "s_waitcnt lgkmcnt(12)\n\t"
    "v_mfma_f32_32x32x16_bf16 v[80:95], v[132:135], v[148:151], v[80:95]\n\t"
    "ds_read_b64_tr_b16 v[132:133], %[vtrA] offset:12448\n\t"
    "ds_read_b64_tr_b16 v[134:135], %[vtrA] offset:13472\n\t"
    "s_waitcnt lgkmcnt(12)\n\t"
    "v_mfma_f32_32x32x16_bf16 v[96:111], v[136:139], v[148:151], v[96:111]\n\t"
    "ds_read_b64_tr_b16 v[136:137], %[vtrA] offset:12480\n\t"
    "ds_read_b64_tr_b16 v[138:139], %[vtrA] offset:13504\n\t"
    "buffer_load_dwordx4 v[148:151], %[vtok], %[shn], s40 offen offset:2048\n\t"
    "s_waitcnt lgkmcnt(12)\n\t"
    "v_mfma_f32_32x32x16_bf16 v[0:15], v[112:115], v[152:155], v[0:15]\n\t"
    "s_waitcnt lgkmcnt(10)\n\t"
    "v_mfma_f32_32x32x16_bf16 v[16:31], v[116:119], v[152:155], v[16:31]\n\t"
    "s_waitcnt lgkmcnt(8)\n\t"
    "v_mfma_f32_32x32x16_bf16 v[32:47], v[120:123], v[152:155], v[32:47]\n\t"
    "s_waitcnt lgkmcnt(6)\n\t"
    "v_mfma_f32_32x32x16_bf16 v[48:63], v[124:127], v[152:155], v[48:63]\n\t"
    "s_waitcnt vmcnt(19) lgkmcnt(0)\n\t"
    "s_barrier\n\t"
    "ds_read_b64_tr_b16 v[112:113], %[vtrA] offset:32896\n\t"
    "ds_read_b64_tr_b16 v[114:115], %[vtrA] offset:33920\n\t"
    "ds_read_b64_tr_b16 v[116:117], %[vtrA] offset:32928\n\t"
    "ds_read_b64_tr_b16 v[118:119], %[vtrA] offset:33952\n\t"
    "v_mfma_f32_32x32x16_bf16 v[64:79], v[128:131], v[152:155], v[64:79]\n\t"
    "ds_read_b64_tr_b16 v[120:121], %[vtrA] offset:32960\n\t"
    "ds_read_b64_tr_b16 v[122:123], %[vtrA] offset:33984\n\t"
    "ds_read_b64_tr_b16 v[124:125], %[vtrA] offset:32992\n\t"
    "ds_read_b64_tr_b16 v[126:127], %[vtrA] offset:34016\n\t"
    "ds_read_b64_tr_b16 v[128:129], %[vtrA] offset:33024\n\t"
    "ds_read_b64_tr_b16 v[130:131], %[vtrA] offset:34048\n\t"
    "s_add_u32 m0, %[sm0], 0\n\t"
    "s_nop 0\n\t"
    "buffer_load_dwordx4 %[vdma], %[sw], s41 offen lds\n\t"
    "v_mfma_f32_32x32x16_bf16 v[80:95], v[132:135], v[152:155], v[80:95]\n\t"
    "ds_read_b64_tr_b16 v[132:133], %[vtrA] offset:33056\n\t"
    "ds_read_b64_tr_b16 v[134:135], %[vtrA] offset:34080\n\t"
    "s_add_u32 m0, %[sm0], 1024\n\t"
    "s_nop 0\n\t"
    "buffer_load_dwordx4 %[vdma], %[sw], s42 offen lds\n\t"
    "s_add_u32 m0, %[sm0], 2048\n\t"
    "s_nop 0\n\t"
    "buffer_load_dwordx4 %[vdma], %[sw], s43 offen lds\n\t"
    "v_mfma_f32_32x32x16_bf16 v[96:111], v[136:139], v[152:155], v[96:111]\n\t"
    "ds_read_b64_tr_b16 v[136:137], %[vtrA] offset:33088\n\t"
    "ds_read_b64_tr_b16 v[138:139], %[vtrA] offset:34112\n\t"
    "s_add_u32 m0, %[sm0], 3072\n\t"
    "s_nop 0\n\t"
    "buffer_load_dwordx4 %[vdma], %[sw], s44 offen lds\n\t"
    "buffer_load_dwordx4 v[152:155], %[vtok], %[shn], s40 offen offset:3072\n\t"
    "s_add_u32 s40, s40, 4096\n\t"
    "s_add_u32 s41, s41, 458752\n\t"
    "s_add_u32 s42, s42, 458752\n\t"
    "s_add_u32 s43, s43, 458752\n\t"
    "s_add_u32 s44, s44, 458752\n\t"
    "s_waitcnt lgkmcnt(12)\n\t"
    "v_mfma_f32_32x32x16_bf16 v[0:15], v[112:115], v[156:159], v[0:15]\n\t"
    "ds_read_b64_tr_b16 v[112:113], %[vtrA] offset:36992\n\t"
    "ds_read_b64_tr_b16 v[114:115], %[vtrA] offset:38016\n\t"
    "s_waitcnt lgkmcnt(12)\n\t"
    "v_mfma_f32_32x32x16_bf16 v[16:31], v[116:119], v[156:159], v[16:31]\n\t"
    "ds_read_b64_tr_b16 v[116:117], %[vtrA] offset:37024\n\t"
    "ds_read_b64_tr_b16 v[118:119], %[vtrA] offset:38048\n\t"
    "s_waitcnt lgkmcnt(12)\n\t"
    "v_mfma_f32_32x32x16_bf16 v[32:47], v[120:123], v[156:159], v[32:47]\n\t"
    "ds_read_b64_tr_b16 v[120:121], %[vtrA] offset:37056\n\t"
    "ds_read_b64_tr_b16 v[122:123], %[vtrA] offset:38080\n\t"
    "s_waitcnt lgkmcnt(12)\n\t"
    "v_mfma_f32_32x32x16_bf16 v[48:63], v[124:127], v[156:159], v[48:63]\n\t"
    "ds_read_b64_tr_b16 v[124:125], %[vtrA] offset:37088\n\t"
    "ds_read_b64_tr_b16 v[126:127], %[vtrA] offset:38112\n\t"
    "s_waitcnt lgkmcnt(12)\n\t"
    "v_mfma_f32_32x32x16_bf16 v[64:79], v[128:131], v[156:159], v[64:79]\n\t"
    "ds_read_b64_tr_b16 v[128:129], %[vtrA] offset:37120\n\t"
    "ds_read_b64_tr_b16 v[130:131], %[vtrA] offset:38144\n\t"
    "s_waitcnt lgkmcnt(12)\n\t"
    "v_mfma_f32_32x32x16_bf16 v[80:95], v[132:135], v[156:159], v[80:95]\n\t"
    "ds_read_b64_tr_b16 v[132:133], %[vtrA] offset:37152\n\t"
    "ds_read_b64_tr_b16 v[134:135], %[vtrA] offset:38176\n\t"
    "s_waitcnt lgkmcnt(12)\n\t"
    "v_mfma_f32_32x32x16_bf16 v[96:111], v[136:139], v[156:159], v[96:111]\n\t"
    "ds_read_b64_tr_b16 v[136:137], %[vtrA] offset:37184\n\t"
    "ds_read_b64_tr_b16 v[138:139], %[vtrA] offset:38208\n\t"
    "buffer_load_dwordx4 v[156:159], %[vtok], %[shn], s40 offen offset:0\n\t"
    "s_waitcnt lgkmcnt(12)\n\t"
    "v_mfma_f32_32x32x16_bf16 v[0:15], v[112:115], v[160:163], v[0:15]\n\t"
    "ds_read_b64_tr_b16 v[112:113], %[vtrA] offset:41088\n\t"
    "ds_read_b64_tr_b16 v[114:115], %[vtrA] offset:42112\n\t"
    "s_waitcnt lgkmcnt(12)\n\t"
    "v_mfma_f32_32x32x16_bf16 v[16:31], v[116:119], v[160:163], v[16:31]\n\t"
    "ds_read_b64_tr_b16 v[116:117], %[vtrA] offset:41120\n\t"
    "ds_read_b64_tr_b16 v[118:119], %[vtrA] offset:42144\n\t"
    "s_waitcnt lgkmcnt(12)\n\t"
    "v_mfma_f32_32x32x16_bf16 v[32:47], v[120:123], v[160:163], v[32:47]\n\t"
    "ds_read_b64_tr_b16 v[120:121], %[vtrA] offset:41152\n\t"
    "ds_read_b64_tr_b16 v[122:123], %[vtrA] offset:42176\n\t"
    "s_waitcnt lgkmcnt(12)\n\t"
    "v_mfma_f32_32x32x16_bf16 v[48:63], v[124:127], v[160:163], v[48:63]\n\t"
    "ds_read_b64_tr_b16 v[124:125], %[vtrA] offset:41184\n\t"
    "ds_read_b64_tr_b16 v[126:127], %[vtrA] offset:42208\n\t"
    "s_waitcnt lgkmcnt(12)\n\t"
    "v_mfma_f32_32x32x16_bf16 v[64:79], v[128:131], v[160:163], v[64:79]\n\t"
    "ds_read_b64_tr_b16 v[128:129], %[vtrA] offset:41216\n\t"
    "ds_read_b64_tr_b16 v[130:131], %[vtrA] offset:42240\n\t"
    "s_waitcnt lgkmcnt(12)\n\t"
    "v_mfma_f32_32x32x16_bf16 v[80:95], v[132:135], v[160:163], v[80:95]\n\t"
    "ds_read_b64_tr_b16 v[132:133], %[vtrA] offset:41248\n\t"
    "ds_read_b64_tr_b16 v[134:135], %[vtrA] offset:42272\n\t"
    "s_waitcnt lgkmcnt(12)\n\t"
    "v_mfma_f32_32x32x16_bf16 v[96:111], v[136:139], v[160:163], v[96:111]\n\t"
    "ds_read_b64_tr_b16 v[136:137], %[vtrA] offset:41280\n\t"
    "ds_read_b64_tr_b16 v[138:139], %[vtrA] offset:42304\n\t"
    "buffer_load_dwordx4 v[160:163], %[vtok], %[shn], s40 offen offset:1024\n\t"
    "s_waitcnt lgkmcnt(12)\n\t"
    "v_mfma_f32_32x32x16_bf16 v[0:15], v[112:115], v[164:167], v[0:15]\n\t"
    "ds_read_b64_tr_b16 v[112:113], %[vtrA] offset:45184\n\t"
    "ds_read_b64_tr_b16 v[114:115], %[vtrA] offset:46208\n\t"
    "s_waitcnt lgkmcnt(12)\n\t"
    "v_mfma_f32_32x32x16_bf16 v[16:31], v[116:119], v[164:167], v[16:31]\n\t"
    "ds_read_b64_tr_b16 v[116:117], %[vtrA] offset:45216\n\t"
    "ds_read_b64_tr_b16 v[118:119], %[vtrA] offset:46240\n\t"
    "s_waitcnt lgkmcnt(12)\n\t"
    "v_mfma_f32_32x32x16_bf16 v[32:47], v[120:123], v[164:167], v[32:47]\n\t"
    "ds_read_b64_tr_b16 v[120:121], %[vtrA] offset:45248\n\t"
    "ds_read_b64_tr_b16 v[122:123], %[vtrA] offset:46272\n\t"
    "s_waitcnt lgkmcnt(12)\n\t"
    "v_mfma_f32_32x32x16_bf16 v[48:63], v[124:127], v[164:167], v[48:63]\n\t"
    "ds_read_b64_tr_b16 v[124:125], %[vtrA] offset:45280\n\t"
    "ds_read_b64_tr_b16 v[126:127], %[vtrA] offset:46304\n\t"
    "s_waitcnt lgkmcnt(12)\n\t"
    "v_mfma_f32_32x32x16_bf16 v[64:79], v[128:131], v[164:167], v[64:79]\n\t"
    "ds_read_b64_tr_b16 v[128:129], %[vtrA] offset:45312\n\t"
    "ds_read_b64_tr_b16 v[130:131], %[vtrA] offset:46336\n\t"
    "s_waitcnt lgkmcnt(12)\n\t"
    "v_mfma_f32_32x32x16_bf16 v[80:95], v[132:135], v[164:167], v[80:95]\n\t"
    "ds_read_b64_tr_b16 v[132:133], %[vtrA] offset:45344\n\t"
    "ds_read_b64_tr_b16 v[134:135], %[vtrA] offset:46368\n\t"
    "s_waitcnt lgkmcnt(12)\n\t"
    "v_mfma_f32_32x32x16_bf16 v[96:111], v[136:139], v[164:167], v[96:111]\n\t"
    "ds_read_b64_tr_b16 v[136:137], %[vtrA] offset:45376\n\t"
    "ds_read_b64_tr_b16 v[138:139], %[vtrA] offset:46400\n\t"
    "buffer_load_dwordx4 v[164:167], %[vtok], %[shn], s40 offen offset:2048\n\t"
    "s_waitcnt lgkmcnt(12)\n\t"
    "v_mfma_f32_32x32x16_bf16 v[0:15], v[112:115], v[168:171], v[0:15]\n\t"
    "s_waitcnt lgkmcnt(10)\n\t"
    "v_mfma_f32_32x32x16_bf16 v[16:31], v[116:119], v[168:171], v[16:31]\n\t"
    "s_waitcnt lgkmcnt(8)\n\t"
    "v_mfma_f32_32x32x16_bf16 v[32:47], v[120:123], v[168:171], v[32:47]\n\t"
    "s_waitcnt lgkmcnt(6)\n\t"
    "v_mfma_f32_32x32x16_bf16 v[48:63], v[124:127], v[168:171], v[48:63]\n\t"
    "s_waitcnt vmcnt(19) lgkmcnt(0)\n\t"
    "s_barrier\n\t"
    "ds_read_b64_tr_b16 v[112:113], %[vtrB] offset:0\n\t"
    "ds_read_b64_tr_b16 v[114:115], %[vtrB] offset:1024\n\t"
    "ds_read_b64_tr_b16 v[116:117], %[vtrB] offset:32\n\t"
    "ds_read_b64_tr_b16 v[118:119], %[vtrB] offset:1056\n\t"
    "v_mfma_f32_32x32x16_bf16 v[64:79], v[128:131], v[168:171], v[64:79]\n\t"
    "ds_read_b64_tr_b16 v[120:121], %[vtrB] offset:64\n\t"
    "ds_read_b64_tr_b16 v[122:123], %[vtrB] offset:1088\n\t"
    "ds_read_b64_tr_b16 v[124:125], %[vtrB] offset:96\n\t"
    "ds_read_b64_tr_b16 v[126:127], %[vtrB] offset:1120\n\t"
    "ds_read_b64_tr_b16 v[128:129], %[vtrB] offset:128\n\t"
    "ds_read_b64_tr_b16 v[130:131], %[vtrB] offset:1152\n\t"
    "s_add_u32 m0, %[sm0], 32896\n\t"
    "s_nop 0\n\t"
    "buffer_load_dwordx4 %[vdma], %[sw], s41 offen lds\n\t"
    "v_mfma_f32_32x32x16_bf16 v[80:95], v[132:135], v[168:171], v[80:95]\n\t"
    "ds_read_b64_tr_b16 v[132:133], %[vtrB] offset:160\n\t"
    "ds_read_b64_tr_b16 v[134:135], %[vtrB] offset:1184\n\t"
    "s_add_u32 m0, %[sm0], 33920\n\t"
    "s_nop 0\n\t"
    "buffer_load_dwordx4 %[vdma], %[sw], s42 offen lds\n\t"
    "s_add_u32 m0, %[sm0], 34944\n\t"
    "s_nop 0\n\t"
    "buffer_load_dwordx4 %[vdma], %[sw], s43 offen lds\n\t"
    "v_mfma_f32_32x32x16_bf16 v[96:111], v[136:139], v[168:171], v[96:111]\n\t"
    "ds_read_b64_tr_b16 v[136:137], %[vtrB] offset:192\n\t"
    "ds_read_b64_tr_b16 v[138:139], %[vtrB] offset:1216\n\t"
    "s_add_u32 m0, %[sm0], 35968\n\t"
    "s_nop 0\n\t"
    "buffer_load_dwordx4 %[vdma], %[sw], s44 offen lds\n\t"
    "buffer_load_dwordx4 v[168:171], %[vtok], %[shn], s40 offen offset:3072\n\t"
    "s_add_u32 s40, s40, 4096\n\t"
    "s_add_u32 s41, s41, 458752\n\t"
    "s_add_u32 s42, s42, 458752\n\t"
    "s_add_u32 s43, s43, 458752\n\t"
    "s_add_u32 s44, s44, 458752\n\t"
    "s_waitcnt lgkmcnt(12)\n\t"
    "v_mfma_f32_32x32x16_bf16 v[0:15], v[112:115], v[172:175], v[0:15]\n\t"
    "ds_read_b64_tr_b16 v[112:113], %[vtrB] offset:4096\n\t"
    "ds_read_b64_tr_b16 v[114:115], %[vtrB] offset:5120\n\t"
    "s_waitcnt lgkmcnt(12)\n\t"
    "v_mfma_f32_32x32x16_bf16 v[16:31], v[116:119], v[172:175], v[16:31]\n\t"
    "ds_read_b64_tr_b16 v[116:117], %[vtrB] offset:4128\n\t"
    "ds_read_b64_tr_b16 v[118:119], %[vtrB] offset:5152\n\t"
    "s_waitcnt lgkmcnt(12)\n\t"
    "v_mfma_f32_32x32x16_bf16 v[32:47], v[120:123], v[172:175], v[32:47]\n\t"
    "ds_read_b64_tr_b16 v[120:121], %[vtrB] offset:4160\n\t"
    "ds_read_b64_tr_b16 v[122:123], %[vtrB] offset:5184\n\t"
    "s_waitcnt lgkmcnt(12)\n\t"
    "v_mfma_f32_32x32x16_bf16 v[48:63], v[124:127], v[172:175], v[48:63]\n\t"
    "ds_read_b64_tr_b16 v[124:125], %[vtrB] offset:4192\n\t"
    "ds_read_b64_tr_b16 v[126:127], %[vtrB] offset:5216\n\t"
    "s_waitcnt lgkmcnt(12)\n\t"
    "v_mfma_f32_32x32x16_bf16 v[64:79], v[128:131], v[172:175], v[64:79]\n\t"
    "ds_read_b64_tr_b16 v[128:129], %[vtrB] offset:4224\n\t"
    "ds_read_b64_tr_b16 v[130:131], %[vtrB] offset:5248\n\t"
    "s_waitcnt lgkmcnt(12)\n\t"
    "v_mfma_f32_32x32x16_bf16 v[80:95], v[132:135], v[172:175], v[80:95]\n\t"
    "ds_read_b64_tr_b16 v[132:133], %[vtrB] offset:4256\n\t"
    "ds_read_b64_tr_b16 v[134:135], %[vtrB] offset:5280\n\t"
    "s_waitcnt lgkmcnt(12)\n\t"
    "v_mfma_f32_32x32x16_bf16 v[96:111], v[136:139], v[172:175], v[96:111]\n\t"
    "ds_read_b64_tr_b16 v[136:137], %[vtrB] offset:4288\n\t"
    "ds_read_b64_tr_b16 v[138:139], %[vtrB] offset:5312\n\t"
    "buffer_load_dwordx4 v[172:175], %[vtok], %[shn], s40 offen offset:0\n\t"
    "s_waitcnt lgkmcnt(12)\n\t"
    "v_mfma_f32_32x32x16_bf16 v[0:15], v[112:115], v[176:179], v[0:15]\n\t"
    "ds_read_b64_tr_b16 v[112:113], %[vtrB] offset:8192\n\t"
    "ds_read_b64_tr_b16 v[114:115], %[vtrB] offset:9216\n\t"
    "s_waitcnt lgkmcnt(12)\n\t"
    "v_mfma_f32_32x32x16_bf16 v[16:31], v[116:119], v[176:179], v[16:31]\n\t"
    "ds_read_b64_tr_b16 v[116:117], %[vtrB] offset:8224\n\t"
    "ds_read_b64_tr_b16 v[118:119], %[vtrB] offset:9248\n\t"
    "s_waitcnt lgkmcnt(12)\n\t"
    "v_mfma_f32_32x32x16_bf16 v[32:47], v[120:123], v[176:179], v[32:47]\n\t"
    "ds_read_b64_tr_b16 v[120:121], %[vtrB] offset:8256\n\t"
    "ds_read_b64_tr_b16 v[122:123], %[vtrB] offset:9280\n\t"
    "s_waitcnt lgkmcnt(12)\n\t"
    "v_mfma_f32_32x32x16_bf16 v[48:63], v[124:127], v[176:179], v[48:63]\n\t"
    "ds_read_b64_tr_b16 v[124:125], %[vtrB] offset:8288\n\t"
    "ds_read_b64_tr_b16 v[126:127], %[vtrB] offset:9312\n\t"
    "s_waitcnt lgkmcnt(12)\n\t"
    "v_mfma_f32_32x32x16_bf16 v[64:79], v[128:131], v[176:179], v[64:79]\n\t"
    "ds_read_b64_tr_b16 v[128:129], %[vtrB] offset:8320\n\t"
    "ds_read_b64_tr_b16 v[130:131], %[vtrB] offset:9344\n\t"
    "s_waitcnt lgkmcnt(12)\n\t"
    "v_mfma_f32_32x32x16_bf16 v[80:95], v[132:135], v[176:179], v[80:95]\n\t"
    "ds_read_b64_tr_b16 v[132:133], %[vtrB] offset:8352\n\t"
    "ds_read_b64_tr_b16 v[134:135], %[vtrB] offset:9376\n\t"
    "s_waitcnt lgkmcnt(12)\n\t"
    "v_mfma_f32_32x32x16_bf16 v[96:111], v[136:139], v[176:179], v[96:111]\n\t"
    "ds_read_b64_tr_b16 v[136:137], %[vtrB] offset:8384\n\t"
    "ds_read_b64_tr_b16 v[138:139], %[vtrB] offset:9408\n\t"
    "buffer_load_dwordx4 v[176:179], %[vtok], %[shn], s40 offen offset:1024\n\t"
    "s_waitcnt lgkmcnt(12)\n\t"
    "v_mfma_f32_32x32x16_bf16 v[0:15], v[112:115], v[180:183], v[0:15]\n\t"
    "ds_read_b64_tr_b16 v[112:113], %[vtrB] offset:12288\n\t"
    "ds_read_b64_tr_b16 v[114:115], %[vtrB] offset:13312\n\t"
    "s_waitcnt lgkmcnt(12)\n\t"
    "v_mfma_f32_32x32x16_bf16 v[16:31], v[116:119], v[180:183], v[16:31]\n\t"
    "ds_read_b64_tr_b16 v[116:117], %[vtrB] offset:12320\n\t"
    "ds_read_b64_tr_b16 v[118:119], %[vtrB] offset:13344\n\t"
    "s_waitcnt lgkmcnt(12)\n\t"
    "v_mfma_f32_32x32x16_bf16 v[32:47], v[120:123], v[180:183], v[32:47]\n\t"
    "ds_read_b64_tr_b16 v[120:121], %[vtrB] offset:12352\n\t"
    "ds_read_b64_tr_b16 v[122:123], %[vtrB] offset:13376\n\t"
    "s_waitcnt lgkmcnt(12)\n\t"
    "v_mfma_f32_32x32x16_bf16 v[48:63], v[124:127], v[180:183], v[48:63]\n\t"
    "ds_read_b64_tr_b16 v[124:125], %[vtrB] offset:12384\n\t"
    "ds_read_b64_tr_b16 v[126:127], %[vtrB] offset:13408\n\t"
    "s_waitcnt lgkmcnt(12)\n\t"
    "v_mfma_f32_32x32x16_bf16 v[64:79], v[128:131], v[180:183], v[64:79]\n\t"
    "ds_read_b64_tr_b16 v[128:129], %[vtrB] offset:12416\n\t"
    "ds_read_b64_tr_b16 v[130:131], %[vtrB] offset:13440\n\t"
    "s_waitcnt lgkmcnt(12)\n\t"
    "v_mfma_f32_32x32x16_bf16 v[80:95], v[132:135], v[180:183], v[80:95]\n\t"
    "ds_read_b64_tr_b16 v[132:133], %[vtrB] offset:12448\n\t"
    "ds_read_b64_tr_b16 v[134:135], %[vtrB] offset:13472\n\t"
    "s_waitcnt lgkmcnt(12)\n\t"
    "v_mfma_f32_32x32x16_bf16 v[96:111], v[136:139], v[180:183], v[96:111]\n\t"
    "ds_read_b64_tr_b16 v[136:137], %[vtrB] offset:12480\n\t"
    "ds_read_b64_tr_b16 v[138:139], %[vtrB] offset:13504\n\t"
    "buffer_load_dwordx4 v[180:183], %[vtok], %[shn], s40 offen offset:2048\n\t"
    "s_waitcnt lgkmcnt(12)\n\t"
    "v_mfma_f32_32x32x16_bf16 v[0:15], v[112:115], v[184:187], v[0:15]\n\t"
    "s_waitcnt lgkmcnt(10)\n\t"
    "v_mfma_f32_32x32x16_bf16 v[16:31], v[116:119], v[184:187], v[16:31]\n\t"
    "s_waitcnt lgkmcnt(8)\n\t"
    "v_mfma_f32_32x32x16_bf16 v[32:47], v[120:123], v[184:187], v[32:47]\n\t"
    "s_waitcnt lgkmcnt(6)\n\t"
    "v_mfma_f32_32x32x16_bf16 v[48:63], v[124:127], v[184:187], v[48:63]\n\t"
    "s_waitcnt vmcnt(19) lgkmcnt(0)\n\t"
    "s_barrier\n\t"
    "ds_read_b64_tr_b16 v[112:113], %[vtrB] offset:32896\n\t"
    "ds_read_b64_tr_b16 v[114:115], %[vtrB] offset:33920\n\t"
    "ds_read_b64_tr_b16 v[116:117], %[vtrB] offset:32928\n\t"
    "ds_read_b64_tr_b16 v[118:119], %[vtrB] offset:33952\n\t"
    "v_mfma_f32_32x32x16_bf16 v[64:79], v[128:131], v[184:187], v[64:79]\n\t"
    "ds_read_b64_tr_b16 v[120:121], %[vtrB] offset:32960\n\t"
    "ds_read_b64_tr_b16 v[122:123], %[vtrB] offset:33984\n\t"
    "ds_read_b64_tr_b16 v[124:125], %[vtrB] offset:32992\n\t"
    "ds_read_b64_tr_b16 v[126:127], %[vtrB] offset:34016\n\t"
    "ds_read_b64_tr_b16 v[128:129], %[vtrB] offset:33024\n\t"
    "ds_read_b64_tr_b16 v[130:131], %[vtrB] offset:34048\n\t"
    "s_add_u32 m0, %[sm0], 65792\n\t"
    "s_nop 0\n\t"
    "buffer_load_dwordx4 %[vdma], %[sw], s41 offen lds\n\t"
    "v_mfma_f32_32x32x16_bf16 v[80:95], v[132:135], v[184:187], v[80:95]\n\t"
    "ds_read_b64_tr_b16 v[132:133], %[vtrB] offset:33056\n\t"
    "ds_read_b64_tr_b16 v[134:135], %[vtrB] offset:34080\n\t"
    "s_add_u32 m0, %[sm0], 66816\n\t"
    "s_nop 0\n\t"
    "buffer_load_dwordx4 %[vdma], %[sw], s42 offen lds\n\t"
    "s_add_u32 m0, %[sm0], 67840\n\t"
    "s_nop 0\n\t"
    "buffer_load_dwordx4 %[vdma], %[sw], s43 offen lds\n\t"
    "v_mfma_f32_32x32x16_bf16 v[96:111], v[136:139], v[184:187], v[96:111]\n\t"
    "ds_read_b64_tr_b16 v[136:137], %[vtrB] offset:33088\n\t"
    "ds_read_b64_tr_b16 v[138:139], %[vtrB] offset:34112\n\t"
    "s_add_u32 m0, %[sm0], 68864\n\t"
    "s_nop 0\n\t"
    "buffer_load_dwordx4 %[vdma], %[sw], s44 offen lds\n\t"
    "buffer_load_dwordx4 v[184:187], %[vtok], %[shn], s40 offen offset:3072\n\t"
    "s_add_u32 s40, s40, 4096\n\t"
    "s_add_u32 s41, s41, 458752\n\t"
    "s_add_u32 s42, s42, 458752\n\t"
    "s_add_u32 s43, s43, 458752\n\t"
    "s_add_u32 s44, s44, 458752\n\t"
    "s_waitcnt lgkmcnt(12)\n\t"
    "v_mfma_f32_32x32x16_bf16 v[0:15], v[112:115], v[188:191], v[0:15]\n\t"
    "ds_read_b64_tr_b16 v[112:113], %[vtrB] offset:36992\n\t"
    "ds_read_b64_tr_b16 v[114:115], %[vtrB] offset:38016\n\t"
    "s_waitcnt lgkmcnt(12)\n\t"
    "v_mfma_f32_32x32x16_bf16 v[16:31], v[116:119], v[188:191], v[16:31]\n\t"
    "ds_read_b64_tr_b16 v[116:117], %[vtrB] offset:37024\n\t"
    "ds_read_b64_tr_b16 v[118:119], %[vtrB] offset:38048\n\t"
    "s_waitcnt lgkmcnt(12)\n\t"
    "v_mfma_f32_32x32x16_bf16 v[32:47], v[120:123], v[188:191], v[32:47]\n\t"
    "ds_read_b64_tr_b16 v[120:121], %[vtrB] offset:37056\n\t"
    "ds_read_b64_tr_b16 v[122:123], %[vtrB] offset:38080\n\t"
    "s_waitcnt lgkmcnt(12)\n\t"
    "v_mfma_f32_32x32x16_bf16 v[48:63], v[124:127], v[188:191], v[48:63]\n\t"
    "ds_read_b64_tr_b16 v[124:125], %[vtrB] offset:37088\n\t"
    "ds_read_b64_tr_b16 v[126:127], %[vtrB] offset:38112\n\t"
    "s_waitcnt lgkmcnt(12)\n\t"
    "v_mfma_f32_32x32x16_bf16 v[64:79], v[128:131], v[188:191], v[64:79]\n\t"
    "ds_read_b64_tr_b16 v[128:129], %[vtrB] offset:37120\n\t"
    "ds_read_b64_tr_b16 v[130:131], %[vtrB] offset:38144\n\t"
    "s_waitcnt lgkmcnt(12)\n\t"
    "v_mfma_f32_32x32x16_bf16 v[80:95], v[132:135], v[188:191], v[80:95]\n\t"
    "ds_read_b64_tr_b16 v[132:133], %[vtrB] offset:37152\n\t"
    "ds_read_b64_tr_b16 v[134:135], %[vtrB] offset:38176\n\t"
    "s_waitcnt lgkmcnt(12)\n\t"
    "v_mfma_f32_32x32x16_bf16 v[96:111], v[136:139], v[188:191], v[96:111]\n\t"
    "ds_read_b64_tr_b16 v[136:137], %[vtrB] offset:37184\n\t"
    "ds_read_b64_tr_b16 v[138:139], %[vtrB] offset:38208\n\t"
    "buffer_load_dwordx4 v[188:191], %[vtok], %[shn], s40 offen offset:0\n\t"
    "s_waitcnt lgkmcnt(12)\n\t"
    "v_mfma_f32_32x32x16_bf16 v[0:15], v[112:115], v[192:195], v[0:15]\n\t"
    "ds_read_b64_tr_b16 v[112:113], %[vtrB] offset:41088\n\t"
    "ds_read_b64_tr_b16 v[114:115], %[vtrB] offset:42112\n\t"
    "s_waitcnt lgkmcnt(12)\n\t"
    "v_mfma_f32_32x32x16_bf16 v[16:31], v[116:119], v[192:195], v[16:31]\n\t"
    "ds_read_b64_tr_b16 v[116:117], %[vtrB] offset:41120\n\t"
    "ds_read_b64_tr_b16 v[118:119], %[vtrB] offset:42144\n\t"
    "s_waitcnt lgkmcnt(12)\n\t"
    "v_mfma_f32_32x32x16_bf16 v[32:47], v[120:123], v[192:195], v[32:47]\n\t"
    "ds_read_b64_tr_b16 v[120:121], %[vtrB] offset:41152\n\t"
    "ds_read_b64_tr_b16 v[122:123], %[vtrB] offset:42176\n\t"
    "s_waitcnt lgkmcnt(12)\n\t"
    "v_mfma_f32_32x32x16_bf16 v[48:63], v[124:127], v[192:195], v[48:63]\n\t"
    "ds_read_b64_tr_b16 v[124:125], %[vtrB] offset:41184\n\t"
    "ds_read_b64_tr_b16 v[126:127], %[vtrB] offset:42208\n\t"
    "s_waitcnt lgkmcnt(12)\n\t"
    "v_mfma_f32_32x32x16_bf16 v[64:79], v[128:131], v[192:195], v[64:79]\n\t"
    "ds_read_b64_tr_b16 v[128:129], %[vtrB] offset:41216\n\t"
    "ds_read_b64_tr_b16 v[130:131], %[vtrB] offset:42240\n\t"
    "s_waitcnt lgkmcnt(12)\n\t"
    "v_mfma_f32_32x32x16_bf16 v[80:95], v[132:135], v[192:195], v[80:95]\n\t"
    "ds_read_b64_tr_b16 v[132:133], %[vtrB] offset:41248\n\t"
    "ds_read_b64_tr_b16 v[134:135], %[vtrB] offset:42272\n\t"
    "s_waitcnt lgkmcnt(12)\n\t"
    "v_mfma_f32_32x32x16_bf16 v[96:111], v[136:139], v[192:195], v[96:111]\n\t"
    "ds_read_b64_tr_b16 v[136:137], %[vtrB] offset:41280\n\t"
    "ds_read_b64_tr_b16 v[138:139], %[vtrB] offset:42304\n\t"
    "buffer_load_dwordx4 v[192:195], %[vtok], %[shn], s40 offen offset:1024\n\t"
    "s_waitcnt lgkmcnt(12)\n\t"
    "v_mfma_f32_32x32x16_bf16 v[0:15], v[112:115], v[196:199], v[0:15]\n\t"
    "ds_read_b64_tr_b16 v[112:113], %[vtrB] offset:45184\n\t"
    "ds_read_b64_tr_b16 v[114:115], %[vtrB] offset:46208\n\t"
    "s_waitcnt lgkmcnt(12)\n\t"
    "v_mfma_f32_32x32x16_bf16 v[16:31], v[116:119], v[196:199], v[16:31]\n\t"
    "ds_read_b64_tr_b16 v[116:117], %[vtrB] offset:45216\n\t"
    "ds_read_b64_tr_b16 v[118:119], %[vtrB] offset:46240\n\t"
    "s_waitcnt lgkmcnt(12)\n\t"
    "v_mfma_f32_32x32x16_bf16 v[32:47], v[120:123], v[196:199], v[32:47]\n\t"
    "ds_read_b64_tr_b16 v[120:121], %[vtrB] offset:45248\n\t"
    "ds_read_b64_tr_b16 v[122:123], %[vtrB] offset:46272\n\t"
    "s_waitcnt lgkmcnt(12)\n\t"
    "v_mfma_f32_32x32x16_bf16 v[48:63], v[124:127], v[196:199], v[48:63]\n\t"
    "ds_read_b64_tr_b16 v[124:125], %[vtrB] offset:45280\n\t"
    "ds_read_b64_tr_b16 v[126:127], %[vtrB] offset:46304\n\t"
    "s_waitcnt lgkmcnt(12)\n\t"
    "v_mfma_f32_32x32x16_bf16 v[64:79], v[128:131], v[196:199], v[64:79]\n\t"
    "ds_read_b64_tr_b16 v[128:129], %[vtrB] offset:45312\n\t"
    "ds_read_b64_tr_b16 v[130:131], %[vtrB] offset:46336\n\t"
    "s_waitcnt lgkmcnt(12)\n\t"
    "v_mfma_f32_32x32x16_bf16 v[80:95], v[132:135], v[196:199], v[80:95]\n\t"
    "ds_read_b64_tr_b16 v[132:133], %[vtrB] offset:45344\n\t"
    "ds_read_b64_tr_b16 v[134:135], %[vtrB] offset:46368\n\t"
    "s_waitcnt lgkmcnt(12)\n\t"
    "v_mfma_f32_32x32x16_bf16 v[96:111], v[136:139], v[196:199], v[96:111]\n\t"
    "ds_read_b64_tr_b16 v[136:137], %[vtrB] offset:45376\n\t"
    "ds_read_b64_tr_b16 v[138:139], %[vtrB] offset:46400\n\t"
    "buffer_load_dwordx4 v[196:199], %[vtok], %[shn], s40 offen offset:2048\n\t"
    "s_waitcnt lgkmcnt(12)\n\t"
    "v_mfma_f32_32x32x16_bf16 v[0:15], v[112:115], v[200:203], v[0:15]\n\t"
    "s_waitcnt lgkmcnt(10)\n\t"
    "v_mfma_f32_32x32x16_bf16 v[16:31], v[116:119], v[200:203], v[16:31]\n\t"
    "s_waitcnt lgkmcnt(8)\n\t"
    "v_mfma_f32_32x32x16_bf16 v[32:47], v[120:123], v[200:203], v[32:47]\n\t"
    "s_waitcnt lgkmcnt(6)\n\t"
    "v_mfma_f32_32x32x16_bf16 v[48:63], v[124:127], v[200:203], v[48:63]\n\t"
    "s_waitcnt vmcnt(19) lgkmcnt(0)\n\t"
    "s_barrier\n\t"
    "ds_read_b64_tr_b16 v[112:113], %[vtrA] offset:0\n\t"
    "ds_read_b64_tr_b16 v[114:115], %[vtrA] offset:1024\n\t"
    "ds_read_b64_tr_b16 v[116:117], %[vtrA] offset:32\n\t"
    "ds_read_b64_tr_b16 v[118:119], %[vtrA] offset:1056\n\t"
    "v_mfma_f32_32x32x16_bf16 v[64:79], v[128:131], v[200:203], v[64:79]\n\t"
    "ds_read_b64_tr_b16 v[120:121], %[vtrA] offset:64\n\t"
    "ds_read_b64_tr_b16 v[122:123], %[vtrA] offset:1088\n\t"
    "ds_read_b64_tr_b16 v[124:125], %[vtrA] offset:96\n\t"
    "ds_read_b64_tr_b16 v[126:127], %[vtrA] offset:1120\n\t"
    "ds_read_b64_tr_b16 v[128:129], %[vtrA] offset:128\n\t"
    "ds_read_b64_tr_b16 v[130:131], %[vtrA] offset:1152\n\t"
    "s_add_u32 m0, %[sm0], 98688\n\t"
    "s_nop 0\n\t"
    "buffer_load_dwordx4 %[vdma], %[sw], s41 offen lds\n\t"
    "v_mfma_f32_32x32x16_bf16 v[80:95], v[132:135], v[200:203], v[80:95]\n\t"
    "ds_read_b64_tr_b16 v[132:133], %[vtrA] offset:160\n\t"
    "ds_read_b64_tr_b16 v[134:135], %[vtrA] offset:1184\n\t"
    "s_add_u32 m0, %[sm0], 99712\n\t"
    "s_nop 0\n\t"
    "buffer_load_dwordx4 %[vdma], %[sw], s42 offen lds\n\t"
    "s_add_u32 m0, %[sm0], 100736\n\t"
    "s_nop 0\n\t"
    "buffer_load_dwordx4 %[vdma], %[sw], s43 offen lds\n\t"
    "v_mfma_f32_32x32x16_bf16 v[96:111], v[136:139], v[200:203], v[96:111]\n\t"
    "ds_read_b64_tr_b16 v[136:137], %[vtrA] offset:192\n\t"
    "ds_read_b64_tr_b16 v[138:139], %[vtrA] offset:1216\n\t"
    "s_add_u32 m0, %[sm0], 101760\n\t"
    "s_nop 0\n\t"
    "buffer_load_dwordx4 %[vdma], %[sw], s44 offen lds\n\t"
    "buffer_load_dwordx4 v[200:203], %[vtok], %[shn], s40 offen offset:3072\n\t"
    "s_add_u32 s40, s40, 4096\n\t"
    "s_add_u32 s41, s41, 458752\n\t"
    "s_add_u32 s42, s42, 458752\n\t"
    "s_add_u32 s43, s43, 458752\n\t"
    "s_add_u32 s44, s44, 458752\n\t"
    "s_sub_u32 s45, s45, 1\n\t"
    "s_cmp_lg_u32 s45, 0\n\t"
    "s_cbranch_scc1 L_g1_loop_%=\n\t"
    "s_waitcnt vmcnt(0) lgkmcnt(0)\n\t"
    "s_nop 15\n\t"
    : "=&{v[0:15]}"(a0), "=&{v[16:31]}"(a1), "=&{v[32:47]}"(a2), "=&{v[48:63]}"(a3), "=&{v[64:79]}"(a4), "=&{v[80:95]}"(a5), "=&{v[96:111]}"(a6)
    : [vtrA] "v"(vtrA), [vtrB] "v"(vtrB), [vtok] "v"(vtok), [vdma] "v"(vdma), [shn] "s"(shn), [sw] "s"(sw), [sm0] "s"(sm0)
    : "memory", "scc", "v112", "v113", "v114", "v115", "v116", "v117", "v118", "v119", "v120", "v121", "v122", "v123", "v124", "v125", "v126", "v127", "v128", "v129", "v130", "v131", "v132", "v133", "v134", "v135", "v136", "v137", "v138", "v139", "v140", "v141", "v142", "v143", "v144", "v145", "v146", "v147", "v148", "v149", "v150", "v151", "v152", "v153", "v154", "v155", "v156", "v157", "v158", "v159", "v160", "v161", "v162", "v163", "v164", "v165", "v166", "v167", "v168", "v169", "v170", "v171", "v172", "v173", "v174", "v175", "v176", "v177", "v178", "v179", "v180", "v181", "v182", "v183", "v184", "v185", "v186", "v187", "v188", "v189", "v190", "v191", "v192", "v193", "v194", "v195", "v196", "v197", "v198", "v199", "v200", "v201", "v202", "v203", "s40", "s41", "s42", "s43", "s44", "s45");
  char* ob = (char*)hm + ((size_t)tt * 224 + pn * 7) * 1024 + lane * 16;
  auto epi = [&](const f32x16& a, int T) {
    u32x4 o = {pk2(geglu(a[0], a[8]), geglu(a[1], a[9])), pk2(geglu(a[2], a[10]), geglu(a[3], a[11])),
               pk2(geglu(a[4], a[12]), geglu(a[5], a[13])), pk2(geglu(a[6], a[14]), geglu(a[7], a[15]))};
    *(u32x4*)(ob + T * 1024) = o;
  };
  epi(a0, 0); epi(a1, 1); epi(a2, 2); epi(a3, 3); epi(a4, 4); epi(a5, 5); epi(a6, 6);
}

constexpr int G2_LDS = 2 * 128 * 272;
__global__ __launch_bounds__(512, 2) void k_gemm2(const u16* __restrict__ hm, const u16* __restrict__ wd, const u16* __restrict__ h, u16* __restrict__ out) {
  extern __shared__ __attribute__((aligned(16))) char lds[];
  const int b = blockIdx.x, tid = threadIdx.x, lane = tid & 63;
  const int w = rfl(tid >> 6);
  const int x = b & 7, idx = b >> 3;
  const int pm = (x >> 1) * 4 + (idx & 3), pn = (x & 1) * 8 + (idx >> 2);
  const int wm = w & 3, wk = w >> 2;
  const int tt = pm * 4 + wm;
  const u32 ldsb = (u32)(size_t)lds;
  const int hh = lane >> 5, blk = (lane >> 4) & 1, qq = (lane & 15) >> 2, p = lane & 3;
  const u32 vb = ldsb + wk * 8192 + (8 * hh + qq) * 128 + 32 * blk + 16 * (p >> 1) + 8 * (p & 1);
  const u32 vtrA = vb + 64 * (qq >> 1), vtrB = vb + 64 * (1 - (qq >> 1));
  const u32 vtok = lane * 16;
  const u32 vdma = (u32)((16 * wm + (lane >> 3)) * 2048 + pn * 128 + (((lane & 7) ^ (4 * ((lane >> 4) & 1))) * 16));
  const __amdgpu_buffer_rsrc_t shm = __builtin_amdgcn_make_buffer_rsrc((void*)((const char*)hm + ((size_t)tt * 224 + wk * 112) * 1024), (short)0, 112 * 1024, 0x00020000);
  const __amdgpu_buffer_rsrc_t sw = __builtin_amdgcn_make_buffer_rsrc((void*)((const char*)wd + (size_t)wk * 1792 * 2048), (short)0, 1792 * 2048, 0x00020000);
  const u32 sm0 = rfl(ldsb + wk * 8192 + wm * 2048);
  f32x16 a0, a1;
  asm volatile(
    "s_mov_b32 s40, 0\n\t"
    "s_mov_b32 s41, 0\n\t"
    "s_mov_b32 s42, 16384\n\t"
    "buffer_load_dwordx4 v[40:43], %[vtok], %[shm], s40 offen offset:0\n\t"
    "buffer_load_dwordx4 v[44:47], %[vtok], %[shm], s40 offen offset:1024\n\t"
    "buffer_load_dwordx4 v[48:51], %[vtok], %[shm], s40 offen offset:2048\n\t"
    "s_add_u32 m0, %[sm0], 0\n\t"
    "s_nop 0\n\t"
    "buffer_load_dwordx4 %[vdma], %[sw], s41 offen lds\n\t"
    "s_add_u32 m0, %[sm0], 1024\n\t"
    "s_nop 0\n\t"
    "buffer_load_dwordx4 %[vdma], %[sw], s42 offen lds\n\t"
    "buffer_load_dwordx4 v[52:55], %[vtok], %[shm], s40 offen offset:3072\n\t"
    "s_add_u32 s40, s40, 4096\n\t"
    "s_add_u32 s41, s41, 131072\n\t"
    "s_add_u32 s42, s42, 131072\n\t"
    "buffer_load_dwordx4 v[56:59], %[vtok], %[shm], s40 offen offset:0\n\t"
    "buffer_load_dwordx4 v[60:63], %[vtok], %[shm], s40 offen offset:1024\n\t"
    "buffer_load_dwordx4 v[64:67], %[vtok], %[shm], s40 offen offset:2048\n\t"
    "s_add_u32 m0, %[sm0], 16384\n\t"
    "s_nop 0\n\t"
    "buffer_load_dwordx4 %[vdma], %[sw], s41 offen lds\n\t"
    "s_add_u32 m0, %[sm0], 17408\n\t"
    "s_nop 0\n\t"
    "buffer_load_dwordx4 %[vdma], %[sw], s42 offen lds\n\t"
    "buffer_load_dwordx4 v[68:71], %[vtok], %[shm], s40 offen offset:3072\n\t"
    "s_add_u32 s40, s40, 4096\n\t"
    "s_add_u32 s41, s41, 131072\n\t"
    "s_add_u32 s42, s42, 131072\n\t"
    "buffer_load_dwordx4 v[72:75], %[vtok], %[shm], s40 offen offset:0\n\t"
    "buffer_load_dwordx4 v[76:79], %[vtok], %[shm], s40 offen offset:1024\n\t"
    "buffer_load_dwordx4 v[80:83], %[vtok], %[shm], s40 offen offset:2048\n\t"
    "s_add_u32 m0, %[sm0], 32768\n\t"
    "s_nop 0\n\t"
    "buffer_load_dwordx4 %[vdma], %[sw], s41 offen lds\n\t"
    "s_add_u32 m0, %[sm0], 33792\n\t"
    "s_nop 0\n\t"
    "buffer_load_dwordx4 %[vdma], %[sw], s42 offen lds\n\t"
    "buffer_load_dwordx4 v[84:87], %[vtok], %[shm], s40 offen offset:3072\n\t"
    "s_add_u32 s40, s40, 4096\n\t"
    "s_add_u32 s41, s41, 131072\n\t"
    "s_add_u32 s42, s42, 131072\n\t"
    "buffer_load_dwordx4 v[88:91], %[vtok], %[shm], s40 offen offset:0\n\t"
    "buffer_load_dwordx4 v[92:95], %[vtok], %[shm], s40 offen offset:1024\n\t"
    "buffer_load_dwordx4 v[96:99], %[vtok], %[shm], s40 offen offset:2048\n\t"
    "s_add_u32 m0, %[sm0], 49152\n\t"
    "s_nop 0\n\t"
    "buffer_load_dwordx4 %[vdma], %[sw], s41 offen lds\n\t"
    "s_add_u32 m0, %[sm0], 50176\n\t"
    "s_nop 0\n\t"
    "buffer_load_dwordx4 %[vdma], %[sw], s42 offen lds\n\t"
    "buffer_load_dwordx4 v[100:103], %[vtok], %[shm], s40 offen offset:3072\n\t"
    "s_add_u32 s40, s40, 4096\n\t"
    "s_add_u32 s41, s41, 131072\n\t"
    "s_add_u32 s42, s42, 131072\n\t"
    "v_mov_b32 v0, 0\n\t"
    "v_mov_b32 v1, 0\n\t"
    "v_mov_b32 v2, 0\n\t"
    "v_mov_b32 v3, 0\n\t"
    "v_mov_b32 v4, 0\n\t"
    "v_mov_b32 v5, 0\n\t"
    "v_mov_b32 v6, 0\n\t"
    "v_mov_b32 v7, 0\n\t"
    "v_mov_b32 v8, 0\n\t"
    "v_mov_b32 v9, 0\n\t"
    "v_mov_b32 v10, 0\n\t"
    "v_mov_b32 v11, 0\n\t"
    "v_mov_b32 v12, 0\n\t"
    "v_mov_b32 v13, 0\n\t"
    "v_mov_b32 v14, 0\n\t"
    "v_mov_b32 v15, 0\n\t"
    "v_mov_b32 v16, 0\n\t"
    "v_mov_b32 v17, 0\n\t"
    "v_mov_b32 v18, 0\n\t"
    "v_mov_b32 v19, 0\n\t"
    "v_mov_b32 v20, 0\n\t"
    "v_mov_b32 v21, 0\n\t"
    "v_mov_b32 v22, 0\n\t"
    "v_mov_b32 v23, 0\n\t"
    "v_mov_b32 v24, 0\n\t"
    "v_mov_b32 v25, 0\n\t"
    "v_mov_b32 v26, 0\n\t"
    "v_mov_b32 v27, 0\n\t"
    "v_mov_b32 v28, 0\n\t"
    "v_mov_b32 v29, 0\n\t"
    "v_mov_b32 v30, 0\n\t"
    "v_mov_b32 v31, 0\n\t"
    "s_mov_b32 s45, 7\n\t"
    "s_waitcnt vmcnt(18)\n\t"
    "s_barrier\n\t"
    "ds_read_b64_tr_b16 v[32:33], %[vtrA] offset:0\n\t"
    "ds_read_b64_tr_b16 v[34:35], %[vtrA] offset:512\n\t"
    "ds_read_b64_tr_b16 v[36:37], %[vtrB] offset:0\n\t"
    "ds_read_b64_tr_b16 v[38:39], %[vtrB] offset:512\n\t"
    "L_g2_loop_%=:\n\t"
    "s_waitcnt lgkmcnt(2)\n\t"
    "v_mfma_f32_32x32x16_bf16 v[0:15], v[32:35], v[40:43], v[0:15]\n\t"
    "ds_read_b64_tr_b16 v[32:33], %[vtrA] offset:2048\n\t"
    "ds_read_b64_tr_b16 v[34:35], %[vtrA] offset:2560\n\t"
    "s_waitcnt lgkmcnt(2)\n\t"
    "v_mfma_f32_32x32x16_bf16 v[16:31], v[36:39], v[40:43], v[16:31]\n\t"
    "ds_read_b64_tr_b16 v[36:37], %[vtrB] offset:2048\n\t"
    "ds_read_b64_tr_b16 v[38:39], %[vtrB] offset:2560\n\t"
    "buffer_load_dwordx4 v[40:43], %[vtok], %[shm], s40 offen offset:0\n\t"
    "s_waitcnt lgkmcnt(2)\n\t"
    "v_mfma_f32_32x32x16_bf16 v[0:15], v[32:35], v[44:47], v[0:15]\n\t"
    "ds_read_b64_tr_b16 v[32:33], %[vtrA] offset:4096\n\t"
    "ds_read_b64_tr_b16 v[34:35], %[vtrA] offset:4608\n\t"
    "s_waitcnt lgkmcnt(2)\n\t"
    "v_mfma_f32_32x32x16_bf16 v[16:31], v[36:39], v[44:47], v[16:31]\n\t"
    "ds_read_b64_tr_b16 v[36:37], %[vtrB] offset:4096\n\t"
    "ds_read_b64_tr_b16 v[38:39], %[vtrB] offset:4608\n\t"
    "buffer_load_dwordx4 v[44:47], %[vtok], %[shm], s40 offen offset:1024\n\t"
    "s_waitcnt lgkmcnt(2)\n\t"
    "v_mfma_f32_32x32x16_bf16 v[0:15], v[32:35], v[48:51], v[0:15]\n\t"
    "ds_read_b64_tr_b16 v[32:33], %[vtrA] offset:6144\n\t"
    "ds_read_b64_tr_b16 v[34:35], %[vtrA] offset:6656\n\t"
    "s_waitcnt lgkmcnt(2)\n\t"
    "v_mfma_f32_32x32x16_bf16 v[16:31], v[36:39], v[48:51], v[16:31]\n\t"
    "ds_read_b64_tr_b16 v[36:37], %[vtrB] offset:6144\n\t"
    "ds_read_b64_tr_b16 v[38:39], %[vtrB] offset:6656\n\t"
    "buffer_load_dwordx4 v[48:51], %[vtok], %[shm], s40 offen offset:2048\n\t"
    "s_waitcnt lgkmcnt(2)\n\t"
    "v_mfma_f32_32x32x16_bf16 v[0:15], v[32:35], v[52:55], v[0:15]\n\t"
    "s_waitcnt vmcnt(15) lgkmcnt(0)\n\t"
    "s_barrier\n\t"
    "ds_read_b64_tr_b16 v[32:33], %[vtrA] offset:16384\n\t"
    "ds_read_b64_tr_b16 v[34:35], %[vtrA] offset:16896\n\t"
    "v_mfma_f32_32x32x16_bf16 v[16:31], v[36:39], v[52:55], v[16:31]\n\t"
    "ds_read_b64_tr_b16 v[36:37], %[vtrB] offset:16384\n\t"
    "ds_read_b64_tr_b16 v[38:39], %[vtrB] offset:16896\n\t"
    "s_add_u32 m0, %[sm0], 0\n\t"
    "s_nop 0\n\t"
    "buffer_load_dwordx4 %[vdma], %[sw], s41 offen lds\n\t"
    "s_add_u32 m0, %[sm0], 1024\n\t"
    "s_nop 0\n\t"
    "buffer_load_dwordx4 %[vdma], %[sw], s42 offen lds\n\t"
    "buffer_load_dwordx4 v[52:55], %[vtok], %[shm], s40 offen offset:3072\n\t"
    "s_add_u32 s40, s40, 4096\n\t"
    "s_add_u32 s41, s41, 131072\n\t"
    "s_add_u32 s42, s42, 131072\n\t"
    "s_waitcnt lgkmcnt(2)\n\t"
    "v_mfma_f32_32x32x16_bf16 v[0:15], v[32:35], v[56:59], v[0:15]\n\t"
    "ds_read_b64_tr_b16 v[32:33], %[vtrA] offset:18432\n\t"
    "ds_read_b64_tr_b16 v[34:35], %[vtrA] offset:18944\n\t"
    "s_waitcnt lgkmcnt(2)\n\t"
    "v_mfma_f32_32x32x16_bf16 v[16:31], v[36:39], v[56:59], v[16:31]\n\t"
    "ds_read_b64_tr_b16 v[36:37], %[vtrB] offset:18432\n\t"
    "ds_read_b64_tr_b16 v[38:39], %[vtrB] offset:18944\n\t"
    "buffer_load_dwordx4 v[56:59], %[vtok], %[shm], s40 offen offset:0\n\t"
    "s_waitcnt lgkmcnt(2)\n\t"
    "v_mfma_f32_32x32x16_bf16 v[0:15], v[32:35], v[60:63], v[0:15]\n\t"
    "ds_read_b64_tr_b16 v[32:33], %[vtrA] offset:20480\n\t"
    "ds_read_b64_tr_b16 v[34:35], %[vtrA] offset:20992\n\t"
    "s_waitcnt lgkmcnt(2)\n\t"
    "v_mfma_f32_32x32x16_bf16 v[16:31], v[36:39], v[60:63], v[16:31]\n\t"
    "ds_read_b64_tr_b16 v[36:37], %[vtrB] offset:20480\n\t"
    "ds_read_b64_tr_b16 v[38:39], %[vtrB] offset:20992\n\t"
    "buffer_load_dwordx4 v[60:63], %[vtok], %[shm], s40 offen offset:1024\n\t"
    "s_waitcnt lgkmcnt(2)\n\t"
    "v_mfma_f32_32x32x16_bf16 v[0:15], v[32:35], v[64:67], v[0:15]\n\t"
    "ds_read_b64_tr_b16 v[32:33], %[vtrA] offset:22528\n\t"
    "ds_read_b64_tr_b16 v[34:35], %[vtrA] offset:23040\n\t"
    "s_waitcnt lgkmcnt(2)\n\t"
    "v_mfma_f32_32x32x16_bf16 v[16:31], v[36:39], v[64:67], v[16:31]\n\t"
    "ds_read_b64_tr_b16 v[36:37], %[vtrB] offset:22528\n\t"
    "ds_read_b64_tr_b16 v[38:39], %[vtrB] offset:23040\n\t"
    "buffer_load_dwordx4 v[64:67], %[vtok], %[shm], s40 offen offset:2048\n\t"
    "s_waitcnt lgkmcnt(2)\n\t"
    "v_mfma_f32_32x32x16_bf16 v[0:15], v[32:35], v[68:71], v[0:15]\n\t"
    "s_waitcnt vmcnt(15) lgkmcnt(0)\n\t"
    "s_barrier\n\t"
    "ds_read_b64_tr_b16 v[32:33], %[vtrA] offset:32768\n\t"
    "ds_read_b64_tr_b16 v[34:35], %[vtrA] offset:33280\n\t"
    "v_mfma_f32_32x32x16_bf16 v[16:31], v[36:39], v[68:71], v[16:31]\n\t"
    "ds_read_b64_tr_b16 v[36:37], %[vtrB] offset:32768\n\t"
    "ds_read_b64_tr_b16 v[38:39], %[vtrB] offset:33280\n\t"
    "s_add_u32 m0, %[sm0], 16384\n\t"
    "s_nop 0\n\t"
    "buffer_load_dwordx4 %[vdma], %[sw], s41 offen lds\n\t"
    "s_add_u32 m0, %[sm0], 17408\n\t"
    "s_nop 0\n\t"
    "buffer_load_dwordx4 %[vdma], %[sw], s42 offen lds\n\t"
    "buffer_load_dwordx4 v[68:71], %[vtok], %[shm], s40 offen offset:3072\n\t"
    "s_add_u32 s40, s40, 4096\n\t"
    "s_add_u32 s41, s41, 131072\n\t"
    "s_add_u32 s42, s42, 131072\n\t"
    "s_waitcnt lgkmcnt(2)\n\t"
    "v_mfma_f32_32x32x16_bf16 v[0:15], v[32:35], v[72:75], v[0:15]\n\t"
    "ds_read_b64_tr_b16 v[32:33], %[vtrA] offset:34816\n\t"
    "ds_read_b64_tr_b16 v[34:35], %[vtrA] offset:35328\n\t"
    "s_waitcnt lgkmcnt(2)\n\t"
    "v_mfma_f32_32x32x16_bf16 v[16:31], v[36:39], v[72:75], v[16:31]\n\t"
    "ds_read_b64_tr_b16 v[36:37], %[vtrB] offset:34816\n\t"
    "ds_read_b64_tr_b16 v[38:39], %[vtrB] offset:35328\n\t"
    "buffer_load_dwordx4 v[72:75], %[vtok], %[shm], s40 offen offset:0\n\t"
    "s_waitcnt lgkmcnt(2)\n\t"
    "v_mfma_f32_32x32x16_bf16 v[0:15], v[32:35], v[76:79], v[0:15]\n\t"
    "ds_read_b64_tr_b16 v[32:33], %[vtrA] offset:36864\n\t"
    "ds_read_b64_tr_b16 v[34:35], %[vtrA] offset:37376\n\t"
    "s_waitcnt lgkmcnt(2)\n\t"
    "v_mfma_f32_32x32x16_bf16 v[16:31], v[36:39], v[76:79], v[16:31]\n\t"
    "ds_read_b64_tr_b16 v[36:37], %[vtrB] offset:36864\n\t"
    "ds_read_b64_tr_b16 v[38:39], %[vtrB] offset:37376\n\t"
    "buffer_load_dwordx4 v[76:79], %[vtok], %[shm], s40 offen offset:1024\n\t"
    "s_waitcnt lgkmcnt(2)\n\t"
    "v_mfma_f32_32x32x16_bf16 v[0:15], v[32:35], v[80:83], v[0:15]\n\t"
    "ds_read_b64_tr_b16 v[32:33], %[vtrA] offset:38912\n\t"
    "ds_read_b64_tr_b16 v[34:35], %[vtrA] offset:39424\n\t"
    "s_waitcnt lgkmcnt(2)\n\t"
    "v_mfma_f32_32x32x16_bf16 v[16:31], v[36:39], v[80:83], v[16:31]\n\t"
    "ds_read_b64_tr_b16 v[36:37], %[vtrB] offset:38912\n\t"
    "ds_read_b64_tr_b16 v[38:39], %[vtrB] offset:39424\n\t"
    "buffer_load_dwordx4 v[80:83], %[vtok], %[shm], s40 offen offset:2048\n\t"
    "s_waitcnt lgkmcnt(2)\n\t"
    "v_mfma_f32_32x32x16_bf16 v[0:15], v[32:35], v[84:87], v[0:15]\n\t"
    "s_waitcnt vmcnt(15) lgkmcnt(0)\n\t"
    "s_barrier\n\t"
    "ds_read_b64_tr_b16 v[32:33], %[vtrA] offset:49152\n\t"
    "ds_read_b64_tr_b16 v[34:35], %[vtrA] offset:49664\n\t"
    "v_mfma_f32_32x32x16_bf16 v[16:31], v[36:39], v[84:87], v[16:31]\n\t"
    "ds_read_b64_tr_b16 v[36:37], %[vtrB] offset:49152\n\t"
    "ds_read_b64_tr_b16 v[38:39], %[vtrB] offset:49664\n\t"
    "s_add_u32 m0, %[sm0], 32768\n\t"
    "s_nop 0\n\t"
    "buffer_load_dwordx4 %[vdma], %[sw], s41 offen lds\n\t"
    "s_add_u32 m0, %[sm0], 33792\n\t"
    "s_nop 0\n\t"
    "buffer_load_dwordx4 %[vdma], %[sw], s42 offen lds\n\t"
    "buffer_load_dwordx4 v[84:87], %[vtok], %[shm], s40 offen offset:3072\n\t"
    "s_add_u32 s40, s40, 4096\n\t"
    "s_add_u32 s41, s41, 131072\n\t"
    "s_add_u32 s42, s42, 131072\n\t"
    "s_waitcnt lgkmcnt(2)\n\t"
    "v_mfma_f32_32x32x16_bf16 v[0:15], v[32:35], v[88:91], v[0:15]\n\t"
    "ds_read_b64_tr_b16 v[32:33], %[vtrA] offset:51200\n\t"
    "ds_read_b64_tr_b16 v[34:35], %[vtrA] offset:51712\n\t"
    "s_waitcnt lgkmcnt(2)\n\t"
    "v_mfma_f32_32x32x16_bf16 v[16:31], v[36:39], v[88:91], v[16:31]\n\t"
    "ds_read_b64_tr_b16 v[36:37], %[vtrB] offset:51200\n\t"
    "ds_read_b64_tr_b16 v[38:39], %[vtrB] offset:51712\n\t"
    "buffer_load_dwordx4 v[88:91], %[vtok], %[shm], s40 offen offset:0\n\t"
    "s_waitcnt lgkmcnt(2)\n\t"
    "v_mfma_f32_32x32x16_bf16 v[0:15], v[32:35], v[92:95], v[0:15]\n\t"
    "ds_read_b64_tr_b16 v[32:33], %[vtrA] offset:53248\n\t"
    "ds_read_b64_tr_b16 v[34:35], %[vtrA] offset:53760\n\t"
    "s_waitcnt lgkmcnt(2)\n\t"
    "v_mfma_f32_32x32x16_bf16 v[16:31], v[36:39], v[92:95], v[16:31]\n\t"
    "ds_read_b64_tr_b16 v[36:37], %[vtrB] offset:53248\n\t"
    "ds_read_b64_tr_b16 v[38:39], %[vtrB] offset:53760\n\t"
    "buffer_load_dwordx4 v[92:95], %[vtok], %[shm], s40 offen offset:1024\n\t"
    "s_waitcnt lgkmcnt(2)\n\t"
    "v_mfma_f32_32x32x16_bf16 v[0:15], v[32:35], v[96:99], v[0:15]\n\t"
    "ds_read_b64_tr_b16 v[32:33], %[vtrA] offset:55296\n\t"
    "ds_read_b64_tr_b16 v[34:35], %[vtrA] offset:55808\n\t"
    "s_waitcnt lgkmcnt(2)\n\t"
    "v_mfma_f32_32x32x16_bf16 v[16:31], v[36:39], v[96:99], v[16:31]\n\t"
    "ds_read_b64_tr_b16 v[36:37], %[vtrB] offset:55296\n\t"
    "ds_read_b64_tr_b16 v[38:39], %[vtrB] offset:55808\n\t"
    "buffer_load_dwordx4 v[96:99], %[vtok], %[shm], s40 offen offset:2048\n\t"
    "s_waitcnt lgkmcnt(2)\n\t"
    "v_mfma_f32_32x32x16_bf16 v[0:15], v[32:35], v[100:103], v[0:15]\n\t"
    "s_waitcnt vmcnt(15) lgkmcnt(0)\n\t"
    "s_barrier\n\t"
    "ds_read_b64_tr_b16 v[32:33], %[vtrA] offset:0\n\t"
    "ds_read_b64_tr_b16 v[34:35], %[vtrA] offset:512\n\t"
    "v_mfma_f32_32x32x16_bf16 v[16:31], v[36:39], v[100:103], v[16:31]\n\t"
    "ds_read_b64_tr_b16 v[36:37], %[vtrB] offset:0\n\t"
    "ds_read_b64_tr_b16 v[38:39], %[vtrB] offset:512\n\t"
    "s_add_u32 m0, %[sm0], 49152\n\t"
    "s_nop 0\n\t"
    "buffer_load_dwordx4 %[vdma], %[sw], s41 offen lds\n\t"
    "s_add_u32 m0, %[sm0], 50176\n\t"
    "s_nop 0\n\t"
    "buffer_load_dwordx4 %[vdma], %[sw], s42 offen lds\n\t"
    "buffer_load_dwordx4 v[100:103], %[vtok], %[shm], s40 offen offset:3072\n\t"
    "s_add_u32 s40, s40, 4096\n\t"
    "s_add_u32 s41, s41, 131072\n\t"
    "s_add_u32 s42, s42, 131072\n\t"
    "s_sub_u32 s45, s45, 1\n\t"
    "s_cmp_lg_u32 s45, 0\n\t"
    "s_cbranch_scc1 L_g2_loop_%=\n\t"
    "s_waitcnt vmcnt(0) lgkmcnt(0)\n\t"
    "s_nop 15\n\t"
    : "=&{v[0:15]}"(a0), "=&{v[16:31]}"(a1)
    : [vtrA] "v"(vtrA), [vtrB] "v"(vtrB), [vtok] "v"(vtok), [vdma] "v"(vdma), [shm] "s"(shm), [sw] "s"(sw), [sm0] "s"(sm0)
    : "memory", "scc", "v32", "v33", "v34", "v35", "v36", "v37", "v38", "v39", "v40", "v41", "v42", "v43", "v44", "v45", "v46", "v47", "v48", "v49", "v50", "v51", "v52", "v53", "v54", "v55", "v56", "v57", "v58", "v59", "v60", "v61", "v62", "v63", "v64", "v65", "v66", "v67", "v68", "v69", "v70", "v71", "v72", "v73", "v74", "v75", "v76", "v77", "v78", "v79", "v80", "v81", "v82", "v83", "v84", "v85", "v86", "v87", "v88", "v89", "v90", "v91", "v92", "v93", "v94", "v95", "v96", "v97", "v98", "v99", "v100", "v101", "v102", "v103", "s40", "s41", "s42", "s43", "s44", "s45");
  __syncthreads();
  {
    char* R = lds + wk * (128 * 272) + (wm * 32 + (lane & 31)) * 272 + hh * 16;
#pragma unroll
    for (int qd = 0; qd < 4; qd++) {
      *(f32x4*)(R + qd * 32) = (f32x4){a0[4 * qd], a0[4 * qd + 1], a0[4 * qd + 2], a0[4 * qd + 3]};
      *(f32x4*)(R + 128 + qd * 32) = (f32x4){a1[4 * qd], a1[4 * qd + 1], a1[4 * qd + 2], a1[4 * qd + 3]};
    }
  }
  const int token = tid >> 2, seg = tid & 3;
  const size_t go = (size_t)(pm * 128 + token) * D_ + pn * 64 + seg * 16;
  const u32x4 h0 = *(const u32x4*)(h + go), h1 = *(const u32x4*)(h + go + 8);
  __syncthreads();
  {
    const char* R = lds + token * 272 + seg * 64;
    float o[16];
#pragma unroll
    for (int i = 0; i < 4; i++) {
      const f32x4 s = *(const f32x4*)(R + i * 16) + *(const f32x4*)(R + 128 * 272 + i * 16);
      o[4 * i] = s[0]; o[4 * i + 1] = s[1]; o[4 * i + 2] = s[2]; o[4 * i + 3] = s[3];
    }
    u32x4 r0, r1;
#pragma unroll
    for (int i = 0; i < 4; i++) {
      r0[i] = pk2(bflo(h0[i]) + rbf(o[2 * i]), bfhi(h0[i]) + rbf(o[2 * i + 1]));
      r1[i] = pk2(bflo(h1[i]) + rbf(o[8 + 2 * i]), bfhi(h1[i]) + rbf(o[8 + 2 * i + 1]));
    }
    *(u32x4*)(out + go) = r0; *(u32x4*)(out + go + 8) = r1;
  }
}

extern "C" void kernel_launch(void* const* d_in, const int* in_sizes, int n_in, void* d_out, int out_size, void* d_ws, size_t ws_size, hipStream_t stream) {
  const u16 *q = (const u16*)d_in[0], *k = (const u16*)d_in[1], *v = (const u16*)d_in[2], *sk = (const u16*)d_in[3], *f = (const u16*)d_in[4], *sD = (const u16*)d_in[5];
  const u16 *wg = (const u16*)d_in[7], *wu = (const u16*)d_in[8], *wd = (const u16*)d_in[9];
  char* ws = (char*)d_ws;
  u16 *kvB = (u16*)ws, *zB = (u16*)(ws + (4u << 20)), *h = (u16*)(ws + (8u << 20)), *hn = (u16*)(ws + (12u << 20)), *hm = (u16*)(ws + (16u << 20));
  hipFuncSetAttribute((const void*)k_conv, hipFuncAttributeMaxDynamicSharedMemorySize, CV_LDS);
  hipFuncSetAttribute((const void*)k_gemm1, hipFuncAttributeMaxDynamicSharedMemorySize, G1_LDS);
  hipFuncSetAttribute((const void*)k_gemm2, hipFuncAttributeMaxDynamicSharedMemorySize, G2_LDS);
  k_kv<<<256, 256, 0, stream>>>(k, v, sk, kvB);
  k_conv<<<256, 512, CV_LDS, stream>>>(kvB, f, sD, zB);
  k_ln<<<256, 256, 0, stream>>>(q, zB, h, hn);
  k_gemm1<<<256, 512, G1_LDS, stream>>>(hn, wg, wu, hm);
  k_gemm2<<<256, 512, G2_LDS, stream>>>(hm, wd, h, (u16*)d_out);
}
```

```cpp
#include <hip/hip_runtime.h>
#include <hip/hip_bf16.h>
#include <stdint.h>

typedef unsigned short u16;
typedef unsigned int u32;
typedef float f32x2 __attribute__((ext_vector_type(2)));
typedef float f32x4 __attribute__((ext_vector_type(4)));
typedef float f32x16 __attribute__((ext_vector_type(16)));
typedef u32 u32x2 __attribute__((ext_vector_type(2)));
typedef u32 u32x3 __attribute__((ext_vector_type(3)));
typedef u32 u32x4 __attribute__((ext_vector_type(4)));
typedef __bf16 bf16x2 __attribute__((ext_vector_type(2)));
typedef __bf16 bf16x8 __attribute__((ext_vector_type(8)));
#define DI __device__ __forceinline__
#define LAS __attribute__((address_space(3)))

constexpr int S_ = 2048, D_ = 1024, F_ = 3584;

DI float bflo(u32 w) { return __uint_as_float(w << 16); }
DI float bfhi(u32 w) { return __uint_as_float(w & 0xffff0000u); }
DI u32 pk2(float lo, float hi) { f32x2 v = {lo, hi}; bf16x2 b = __builtin_convertvector(v, bf16x2); return __builtin_bit_cast(u32, b); }
DI float rbf(float x) { return bflo(pk2(x, x)); }
template <class T> DI T ldnt(const void* p) { return __builtin_nontemporal_load((const T*)p); }
DI u32 rfl(u32 x) { return __builtin_amdgcn_readfirstlane(x); }

typedef __attribute__((address_space(1))) u32 gu32;
constexpr int FL_B2 = 0;
constexpr int FL_B3 = 512;
constexpr int FL_B4 = 1536;
constexpr int FL_X = 2560;
constexpr int CV_ECH = 2064 * 8;
constexpr int CV_XO = 2368, CV_XCH = 4752;
constexpr int CV_X0 = 4 * CV_ECH;
constexpr int CV_Z0 = CV_X0 + 4 * CV_XCH;
constexpr int LN_S0 = 98304;
constexpr int LN_H0 = LN_S0 + 16512;
constexpr int LN_RED = 131328;
constexpr int CODE_DUMP = 132096;
constexpr int GM_LDS = 135168;

DI void st16_dev(void* p, u32x4 v) { asm volatile("global_store_dwordx4 %0, %1, off sc1\n\ts_nop 1" :: "v"(p), "v"(v) : "memory"); }
DI void drain_and_barrier() { asm volatile("s_waitcnt vmcnt(0) lgkmcnt(0)\n\ts_barrier" ::: "memory"); }
DI void arrive(u32* c) { if (threadIdx.x == 0) __hip_atomic_fetch_add((gu32*)c, 1u, __ATOMIC_RELAXED, __HIP_MEMORY_SCOPE_AGENT); }
DI void wait_counters(const u32* c, int n, int stride, u32 want) {
  if (threadIdx.x < 64) {
    const int l = threadIdx.x;
    const gu32* p = (const gu32*)(c + (l < n ? l : 0) * stride);
    u32 spins = 0;
#pragma clang loop unroll(disable)
    while (true) {
      const u32 v = __hip_atomic_load(p, __ATOMIC_RELAXED, __HIP_MEMORY_SCOPE_AGENT);
      if (__all(v >= want)) break;
      __builtin_amdgcn_s_sleep(1);
      if (++spins > (1u << 17)) break;
    }
  }
  asm volatile("s_waitcnt vmcnt(0) lgkmcnt(0)\n\ts_barrier" ::: "memory");
}

DI float swap_add32(float a, float b) {
  auto r = __builtin_amdgcn_permlane32_swap(__float_as_uint(a), __float_as_uint(b), false, false);
  return __uint_as_float(r[0]) + __uint_as_float(r[1]);
}
DI float swap_add16(float a, float b) {
  auto r = __builtin_amdgcn_permlane16_swap(__float_as_uint(a), __float_as_uint(b), false, false);
  return __uint_as_float(r[0]) + __uint_as_float(r[1]);
}
template <int CTRL> DI float dppv(float v) { return __int_as_float(__builtin_amdgcn_update_dpp(0, __float_as_int(v), CTRL, 0xf, 0xf, true)); }
DI float reduce8(const float (&v)[8], bool hi8) {
  const float a0 = swap_add32(v[0], v[4]), a1 = swap_add32(v[1], v[5]), a2 = swap_add32(v[2], v[6]), a3 = swap_add32(v[3], v[7]);
  const float b0 = swap_add16(a0, a2), b1 = swap_add16(a1, a3);
  const float keep = hi8 ? b1 : b0, send = hi8 ? b0 : b1;
  float c = keep + dppv<0x128>(send);
  c += dppv<0xb1>(c);
  c += dppv<0x4e>(c);
  c += dppv<0x141>(c);
  return c;
}
DI void row_stats(const float (&s1)[8], const float (&s2)[8], const float pv, float* red, int w, int lane, float (&mb)[8], float (&rs)[8]) {
  const bool hi8 = (lane >> 3) & 1;
  const float t1 = reduce8(s1, hi8), t2 = reduce8(s2, hi8);
  const int rr = 4 * (lane >> 5) + 2 * ((lane >> 4) & 1) + ((lane >> 3) & 1);
  if ((lane & 7) == 0) *(f32x2*)(red + (w * 8 + rr) * 2) = (f32x2){t1, t2};
  asm volatile("s_waitcnt lgkmcnt(0)\n\ts_barrier" ::: "memory");
  f32x2 p = *(const f32x2*)(red + (lane & 31) * 2);
  p[0] += dppv<0x128>(p[0]); p[1] += dppv<0x128>(p[1]);
  { auto r0 = __builtin_amdgcn_permlane16_swap(__float_as_uint(p[0]), __float_as_uint(p[0]), false, false); p[0] = __uint_as_float(r0[0]) + __uint_as_float(r0[1]);
    auto r1 = __builtin_amdgcn_permlane16_swap(__float_as_uint(p[1]), __float_as_uint(p[1]), false, false); p[1] = __uint_as_float(r1[0]) + __uint_as_float(r1[1]); }
  const float md = p[0] * (1.f / D_);
  const float var = fmaxf(p[1] * (1.f / D_) - md * md, 0.f);
  const float m = rbf(pv + md);
  const float sd = rbf(__builtin_amdgcn_sqrtf(rbf(rbf(var) + 1e-5f)));
  const float ri = __builtin_amdgcn_rcpf(sd);
#pragma unroll
  for (int r = 0; r < 8; r++) {
    mb[r] = __int_as_float(__builtin_amdgcn_readlane(__float_as_int(m), r));
    rs[r] = __int_as_float(__builtin_amdgcn_readlane(__float_as_int(ri), r));
  }
}
DI u32 geglu2(f32x2 g, f32x2 uu, f32x2 C1, f32x2 C3, f32x2 ONE) {
  const f32x2 p = __builtin_elementwise_fma(g * g, C3, C1);
  const f32x2 a = g * p;
  const f32x2 e = {__builtin_amdgcn_exp2f(a[0]), __builtin_amdgcn_exp2f(a[1])};
  const f32x2 d = e + ONE;
  const f32x2 r = {__builtin_amdgcn_rcpf(d[0]), __builtin_amdgcn_rcpf(d[1])};
  const f32x2 o = (g * uu) * r;
  return pk2(o[0], o[1]);
}

constexpr int CODE_BYTES = 32768;
DI const char* main_code() {
  const char* p;
  asm volatile("s_getpc_b64 s[40:41]\n\ts_add_u32 s40, s40, k_main@rel32@lo+4\n\ts_addc_u32 s41, s41, k_main@rel32@hi+12\n\ts_mov_b64 %0, s[40:41]" : "=s"(p) :: "s40", "s41", "scc");
  return p;
}

struct Params {
  const u16 *x, *f, *sD, *kvB, *wg, *wu, *wd;
  u16 *zB, *h, *hn, *hm, *out;
  float* xch;
  u32* flags;
};

#define G2_PREFETCH() do { \
    const int kh_ = xcd & 1, pn_ = ((xcd >> 1) & 1) * 4 + (li >> 3); \
    const u32 drow_ = (u32)((16 * w + (lane >> 4)) * 2048 + pn_ * 256); \
    const u32 v0_ = drow_ + dsw0, v1_ = drow_ + dsw1 + 7168, v2_ = v0_ + 2 * 7168, v3_ = v1_ + 2 * 7168; \
    const __amdgpu_buffer_rsrc_t sw_ = __builtin_amdgcn_make_buffer_rsrc((void*)((const char*)P.wd + (size_t)kh_ * 1792 * 2048), (short)0, 1792 * 2048, 0x00020000); \
    const u32 sm_ = rfl(ldsb + w * 4096); \
    asm volatile( \
    "s_mov_b32 s42, 0\n\t" \
    "s_add_u32 m0, %[sm0], 0\n\t" \
    "s_add_u32 s43, s42, 0\n\t" \
    "buffer_load_dwordx4 %[vdma0], %[sw], s43 offen offset:0 lds\n\t" \
    "buffer_load_dwordx4 %[vdma1], %[sw], s43 offen offset:1024 lds\n\t" \
    "buffer_load_dwordx4 %[vdma2], %[sw], s43 offen offset:2048 lds\n\t" \
    "buffer_load_dwordx4 %[vdma3], %[sw], s43 offen offset:3072 lds\n\t" \
    "s_add_u32 s42, s42, 131072\n\t" \
    "s_add_u32 m0, %[sm0], 16384\n\t" \
    "s_add_u32 s43, s42, 0\n\t" \
    "buffer_load_dwordx4 %[vdma0], %[sw], s43 offen offset:0 lds\n\t" \
    "buffer_load_dwordx4 %[vdma1], %[sw], s43 offen offset:1024 lds\n\t" \
    "buffer_load_dwordx4 %[vdma2], %[sw], s43 offen offset:2048 lds\n\t" \
    "buffer_load_dwordx4 %[vdma3], %[sw], s43 offen offset:3072 lds\n\t" \
    "s_add_u32 s42, s42, 131072\n\t" \
    "s_add_u32 m0, %[sm0], 32768\n\t" \
    "s_add_u32 s43, s42, 0\n\t" \
    "buffer_load_dwordx4 %[vdma0], %[sw], s43 offen offset:0 lds\n\t" \
    "buffer_load_dwordx4 %[vdma1], %[sw], s43 offen offset:1024 lds\n\t" \
    "buffer_load_dwordx4 %[vdma2], %[sw], s43 offen offset:2048 lds\n\t" \
    "buffer_load_dwordx4 %[vdma3], %[sw], s43 offen offset:3072 lds\n\t" \
    "s_add_u32 s42, s42, 131072\n\t" \
      :: [vdma0] "v"(v0_), [vdma1] "v"(v1_), [vdma2] "v"(v2_), [vdma3] "v"(v3_), [sw] "s"(sw_), [sm0] "s"(sm_) : "memory", "scc", "s42", "s43"); \
  } while (0)

extern "C" __global__ __launch_bounds__(256, 1) void k_main(const Params P) {
  extern __shared__ __attribute__((aligned(256))) char lds[];
  const int b = blockIdx.x, tid = threadIdx.x, lane = tid & 63;
  const int w = rfl(tid >> 6);
  const int xcd = b & 7, li = b >> 3;
  const u32 ldsb = (u32)(size_t)lds;
  const int u = xcd * 32 + li;
  u32x2 xx[8];
  float pvl;
  {
    const int c0 = u * 4;
    const __amdgpu_buffer_rsrc_t rf = __builtin_amdgcn_make_buffer_rsrc((void*)P.f, (short)0, D_ * S_ * 2, 0x00020000);
    u32x4 xp[4];
#pragma unroll
    for (int i = 0; i < 4; i++) {
      const int idx = tid + 256 * i, uu = idx >> 2, c = idx & 3;
      xp[i] = *(const u32x4*)(P.kvB + ((size_t)uu * D_ + c0 + c) * 8);
    }
    u32x3 fd[16];
#pragma unroll
    for (int i = 0; i < 16; i++) {
      const int idx = tid + 256 * i, c = idx >> 10, l = idx & 1023;
      fd[i] = __builtin_amdgcn_raw_buffer_load_b96(rf, (u32)(((c0 + c) * 1024 + l) * 4), 0, 0);
    }
    if (w == 3) {
      const __amdgpu_buffer_rsrc_t rc = __builtin_amdgcn_make_buffer_rsrc((void*)main_code(), (short)0, CODE_BYTES, 0x00020000);
      const u32 vo = (u32)(li * 2048 + lane * 16), m0v = rfl(ldsb + CODE_DUMP);
      asm volatile("s_mov_b32 m0, %2\n\ts_nop 0\n\tbuffer_load_dwordx4 %0, %1, 0 offen lds\n\tbuffer_load_dwordx4 %0, %1, 0 offen offset:1024 lds" :: "v"(vo), "s"(rc), "s"(m0v) : "memory");
    }
    if (tid < 128) {
      const int c = tid >> 5, par = (tid >> 4) & 1, vv = tid & 15;
      *(u32x4*)(lds + CV_X0 + c * CV_XCH + par * CV_XO + vv * 16) = (u32x4){0u, 0u, 0u, 0u};
    } else if (tid < 160) {
      const int c = (tid - 128) >> 3, l = ((tid - 128) & 7) - 8;
      const u32* fr = (const u32*)(P.f + (size_t)(c0 + c) * S_);
      const u32 d0 = 0, d1 = (l + 1 >= 0) ? fr[0] : 0u, d2 = (l + 2 >= 0) ? fr[l + 2] : 0u;
      *(u32x4*)(lds + c * CV_ECH + (2 * l + 16) * 8) = (u32x4){d0, d1, __builtin_amdgcn_alignbit(d1, d0, 16), __builtin_amdgcn_alignbit(d2, d1, 16)};
    }
#pragma unroll
    for (int i = 0; i < 4; i++) {
      const int idx = tid + 256 * i, uu = idx >> 2, c = idx & 3;
      *(u32x4*)(lds + CV_X0 + c * CV_XCH + (uu & 1) * CV_XO + ((uu >> 1) + 16) * 16) = xp[i];
    }
#pragma unroll
    for (int i = 0; i < 16; i++) {
      const int idx = tid + 256 * i, c = idx >> 10, l = idx & 1023;
      *(u32x4*)(lds + c * CV_ECH + (2 * l + 16) * 8) = (u32x4){fd[i][0], fd[i][1], __builtin_amdgcn_alignbit(fd[i][1], fd[i][0], 16), __builtin_amdgcn_alignbit(fd[i][2], fd[i][1], 16)};
    }
    const float dd = bflo((u32)P.sD[c0 + w]);
#pragma unroll
    for (int r = 0; r < 8; r++) xx[r] = ldnt<u32x2>(P.x + (size_t)(u * 8 + r) * D_ + 4 * tid);
    pvl = bflo((u32)P.x[(size_t)(u * 8 + (lane & 7)) * D_]);
    asm volatile("s_waitcnt lgkmcnt(0)\n\ts_barrier" ::: "memory");
    const int c = w, lb = lane & 15, g = lane >> 4;
    const int kap = 16 * (g & 1) + 8 * (g >> 1);
    f32x16 aA, aB;
    {
      u32 va = ldsb + c * CV_ECH + (16 + lb - kap - 7 + 16) * 8;
      u32 vb = ldsb + CV_X0 + c * CV_XCH + (g >> 1) * CV_XO + (lb - 1 + (g & 1) + 16) * 16 - 4 * 16;
      asm volatile(
    "ds_read_b128 a[60:63], %[vb] offset:1856\n\t"
    "ds_read_b64 a[0:1], %[va] offset:0\n\t"
    "ds_read_b64 a[2:3], %[va] offset:32\n\t"
    "ds_read_b128 a[56:59], %[vb] offset:1600\n\t"
    "ds_read_b64 a[4:5], %[va] offset:2048\n\t"
    "ds_read_b64 a[6:7], %[va] offset:2080\n\t"
    "ds_read_b128 a[52:55], %[vb] offset:1344\n\t"
    "ds_read_b64 a[8:9], %[va] offset:4096\n\t"
    "ds_read_b64 a[10:11], %[va] offset:4128\n\t"
    "ds_read_b128 a[48:51], %[vb] offset:1088\n\t"
    "ds_read_b64 a[12:13], %[va] offset:6144\n\t"
    "ds_read_b64 a[14:15], %[va] offset:6176\n\t"
    "s_waitcnt lgkmcnt(4)\n\t"
    "ds_read_b128 a[44:47], %[vb] offset:832\n\t"
    "ds_read_b64 a[16:17], %[va] offset:8192\n\t"
    "ds_read_b64 a[18:19], %[va] offset:8224\n\t"
    "ds_read_b128 a[40:43], %[vb] offset:576\n\t"
    "ds_read_b64 a[20:21], %[va] offset:10240\n\t"
    "ds_read_b64 a[22:23], %[va] offset:10272\n\t"
    "ds_read_b128 a[36:39], %[vb] offset:320\n\t"
    "ds_read_b64 a[24:25], %[va] offset:12288\n\t"
    "ds_read_b64 a[26:27], %[va] offset:12320\n\t"
    "ds_read_b128 a[32:35], %[vb] offset:64\n\t"
    "ds_read_b64 a[28:29], %[va] offset:14336\n\t"
    "ds_read_b64 a[30:31], %[va] offset:14368\n\t"
    "v_mov_b32 v0, 0\n\t"
    "v_mov_b32 v1, 0\n\t"
    "v_mov_b32 v2, 0\n\t"
    "v_mov_b32 v3, 0\n\t"
    "v_mov_b32 v4, 0\n\t"
    "v_mov_b32 v5, 0\n\t"
    "v_mov_b32 v6, 0\n\t"
    "v_mov_b32 v7, 0\n\t"
    "v_mov_b32 v8, 0\n\t"
    "v_mov_b32 v9, 0\n\t"
    "v_mov_b32 v10, 0\n\t"
    "v_mov_b32 v11, 0\n\t"
    "v_mov_b32 v12, 0\n\t"
    "v_mov_b32 v13, 0\n\t"
    "v_mov_b32 v14, 0\n\t"
    "v_mov_b32 v15, 0\n\t"
    "v_mov_b32 v16, 0\n\t"
    "v_mov_b32 v17, 0\n\t"
    "v_mov_b32 v18, 0\n\t"
    "v_mov_b32 v19, 0\n\t"
    "v_mov_b32 v20, 0\n\t"
    "v_mov_b32 v21, 0\n\t"
    "v_mov_b32 v22, 0\n\t"
    "v_mov_b32 v23, 0\n\t"
    "v_mov_b32 v24, 0\n\t"
    "v_mov_b32 v25, 0\n\t"
    "v_mov_b32 v26, 0\n\t"
    "v_mov_b32 v27, 0\n\t"
    "v_mov_b32 v28, 0\n\t"
    "v_mov_b32 v29, 0\n\t"
    "v_mov_b32 v30, 0\n\t"
    "v_mov_b32 v31, 0\n\t"
    "s_mov_b32 s45, 4\n\t"
    "L_cv_loop_%=:\n\t"
    "s_waitcnt lgkmcnt(0)\n\t"
    "v_mfma_f32_16x16x32_bf16 v[28:31], a[0:3], a[60:63], v[28:31]\n\t"
    "ds_read_b128 a[124:127], %[vb] offset:1824\n\t"
    "v_mfma_f32_16x16x32_bf16 v[28:31], a[4:7], a[56:59], v[28:31]\n\t"
    "ds_read_b64 a[64:65], %[va] offset:256\n\t"
    "v_mfma_f32_16x16x32_bf16 v[28:31], a[8:11], a[52:55], v[28:31]\n\t"
    "ds_read_b64 a[66:67], %[va] offset:288\n\t"
    "v_mfma_f32_16x16x32_bf16 v[28:31], a[12:15], a[48:51], v[28:31]\n\t"
    "ds_read_b128 a[120:123], %[vb] offset:1568\n\t"
    "v_mfma_f32_16x16x32_bf16 v[28:31], a[16:19], a[44:47], v[28:31]\n\t"
    "ds_read_b64 a[68:69], %[va] offset:2304\n\t"
    "v_mfma_f32_16x16x32_bf16 v[28:31], a[20:23], a[40:43], v[28:31]\n\t"
    "ds_read_b64 a[70:71], %[va] offset:2336\n\t"
    "v_mfma_f32_16x16x32_bf16 v[28:31], a[24:27], a[36:39], v[28:31]\n\t"
    "ds_read_b128 a[116:119], %[vb] offset:1312\n\t"
    "v_mfma_f32_16x16x32_bf16 v[28:31], a[28:31], a[32:35], v[28:31]\n\t"
    "ds_read_b64 a[72:73], %[va] offset:4352\n\t"
    "v_mfma_f32_16x16x32_bf16 v[24:27], a[0:3], a[56:59], v[24:27]\n\t"
    "ds_read_b64 a[74:75], %[va] offset:4384\n\t"
    "v_mfma_f32_16x16x32_bf16 v[24:27], a[4:7], a[52:55], v[24:27]\n\t"
    "ds_read_b128 a[112:115], %[vb] offset:1056\n\t"
    "v_mfma_f32_16x16x32_bf16 v[24:27], a[8:11], a[48:51], v[24:27]\n\t"
    "ds_read_b64 a[76:77], %[va] offset:6400\n\t"
    "v_mfma_f32_16x16x32_bf16 v[24:27], a[12:15], a[44:47], v[24:27]\n\t"
    "ds_read_b64 a[78:79], %[va] offset:6432\n\t"
    "v_mfma_f32_16x16x32_bf16 v[24:27], a[16:19], a[40:43], v[24:27]\n\t"
    "v_mfma_f32_16x16x32_bf16 v[24:27], a[20:23], a[36:39], v[24:27]\n\t"
    "ds_read_b128 a[108:111], %[vb] offset:800\n\t"
    "v_mfma_f32_16x16x32_bf16 v[24:27], a[24:27], a[32:35], v[24:27]\n\t"
    "ds_read_b64 a[80:81], %[va] offset:8448\n\t"
    "v_mfma_f32_16x16x32_bf16 v[20:23], a[0:3], a[52:55], v[20:23]\n\t"
    "ds_read_b64 a[82:83], %[va] offset:8480\n\t"
    "v_mfma_f32_16x16x32_bf16 v[20:23], a[4:7], a[48:51], v[20:23]\n\t"
    "ds_read_b128 a[104:107], %[vb] offset:544\n\t"
    "v_mfma_f32_16x16x32_bf16 v[20:23], a[8:11], a[44:47], v[20:23]\n\t"
    "ds_read_b64 a[84:85], %[va] offset:10496\n\t"
    "v_mfma_f32_16x16x32_bf16 v[20:23], a[12:15], a[40:43], v[20:23]\n\t"
    "ds_read_b64 a[86:87], %[va] offset:10528\n\t"
    "v_mfma_f32_16x16x32_bf16 v[20:23], a[16:19], a[36:39], v[20:23]\n\t"
    "ds_read_b128 a[100:103], %[vb] offset:288\n\t"
    "v_mfma_f32_16x16x32_bf16 v[20:23], a[20:23], a[32:35], v[20:23]\n\t"
    "ds_read_b64 a[88:89], %[va] offset:12544\n\t"
    "v_mfma_f32_16x16x32_bf16 v[16:19], a[0:3], a[48:51], v[16:19]\n\t"
    "ds_read_b64 a[90:91], %[va] offset:12576\n\t"
    "v_mfma_f32_16x16x32_bf16 v[16:19], a[4:7], a[44:47], v[16:19]\n\t"
    "ds_read_b128 a[96:99], %[vb] offset:32\n\t"
    "v_mfma_f32_16x16x32_bf16 v[16:19], a[8:11], a[40:43], v[16:19]\n\t"
    "ds_read_b64 a[92:93], %[va] offset:14592\n\t"
    "v_mfma_f32_16x16x32_bf16 v[16:19], a[12:15], a[36:39], v[16:19]\n\t"
    "ds_read_b64 a[94:95], %[va] offset:14624\n\t"
    "v_mfma_f32_16x16x32_bf16 v[16:19], a[16:19], a[32:35], v[16:19]\n\t"
    "v_mfma_f32_16x16x32_bf16 v[12:15], a[0:3], a[44:47], v[12:15]\n\t"
    "v_mfma_f32_16x16x32_bf16 v[12:15], a[4:7], a[40:43], v[12:15]\n\t"
    "v_mfma_f32_16x16x32_bf16 v[12:15], a[8:11], a[36:39], v[12:15]\n\t"
    "v_mfma_f32_16x16x32_bf16 v[12:15], a[12:15], a[32:35], v[12:15]\n\t"
    "v_mfma_f32_16x16x32_bf16 v[8:11], a[0:3], a[40:43], v[8:11]\n\t"
    "v_mfma_f32_16x16x32_bf16 v[8:11], a[4:7], a[36:39], v[8:11]\n\t"
    "v_mfma_f32_16x16x32_bf16 v[8:11], a[8:11], a[32:35], v[8:11]\n\t"
    "v_mfma_f32_16x16x32_bf16 v[4:7], a[0:3], a[36:39], v[4:7]\n\t"
    "v_mfma_f32_16x16x32_bf16 v[4:7], a[4:7], a[32:35], v[4:7]\n\t"
    "v_mfma_f32_16x16x32_bf16 v[0:3], a[0:3], a[32:35], v[0:3]\n\t"
    "s_waitcnt lgkmcnt(0)\n\t"
    "v_mfma_f32_16x16x32_bf16 v[28:31], a[64:67], a[124:127], v[28:31]\n\t"
    "ds_read_b128 a[60:63], %[vb] offset:1792\n\t"
    "v_mfma_f32_16x16x32_bf16 v[28:31], a[68:71], a[120:123], v[28:31]\n\t"
    "ds_read_b64 a[0:1], %[va] offset:512\n\t"
    "v_mfma_f32_16x16x32_bf16 v[28:31], a[72:75], a[116:119], v[28:31]\n\t"
    "ds_read_b64 a[2:3], %[va] offset:544\n\t"
    "v_mfma_f32_16x16x32_bf16 v[28:31], a[76:79], a[112:115], v[28:31]\n\t"
    "ds_read_b128 a[56:59], %[vb] offset:1536\n\t"
    "v_mfma_f32_16x16x32_bf16 v[28:31], a[80:83], a[108:111], v[28:31]\n\t"
    "ds_read_b64 a[4:5], %[va] offset:2560\n\t"
    "v_mfma_f32_16x16x32_bf16 v[28:31], a[84:87], a[104:107], v[28:31]\n\t"
    "ds_read_b64 a[6:7], %[va] offset:2592\n\t"
    "v_mfma_f32_16x16x32_bf16 v[28:31], a[88:91], a[100:103], v[28:31]\n\t"
    "ds_read_b128 a[52:55], %[vb] offset:1280\n\t"
    "v_mfma_f32_16x16x32_bf16 v[28:31], a[92:95], a[96:99], v[28:31]\n\t"
    "ds_read_b64 a[8:9], %[va] offset:4608\n\t"
    "v_mfma_f32_16x16x32_bf16 v[24:27], a[64:67], a[120:123], v[24:27]\n\t"
    "ds_read_b64 a[10:11], %[va] offset:4640\n\t"
    "v_mfma_f32_16x16x32_bf16 v[24:27], a[68:71], a[116:119], v[24:27]\n\t"
    "ds_read_b128 a[48:51], %[vb] offset:1024\n\t"
    "v_mfma_f32_16x16x32_bf16 v[24:27], a[72:75], a[112:115], v[24:27]\n\t"
    "ds_read_b64 a[12:13], %[va] offset:6656\n\t"
    "v_mfma_f32_16x16x32_bf16 v[24:27], a[76:79], a[108:111], v[24:27]\n\t"
    "ds_read_b64 a[14:15], %[va] offset:6688\n\t"
    "v_mfma_f32_16x16x32_bf16 v[24:27], a[80:83], a[104:107], v[24:27]\n\t"
    "v_mfma_f32_16x16x32_bf16 v[24:27], a[84:87], a[100:103], v[24:27]\n\t"
    "ds_read_b128 a[44:47], %[vb] offset:768\n\t"
    "v_mfma_f32_16x16x32_bf16 v[24:27], a[88:91], a[96:99], v[24:27]\n\t"
    "ds_read_b64 a[16:17], %[va] offset:8704\n\t"
    "v_mfma_f32_16x16x32_bf16 v[20:23], a[64:67], a[116:119], v[20:23]\n\t"
    "ds_read_b64 a[18:19], %[va] offset:8736\n\t"
    "v_mfma_f32_16x16x32_bf16 v[20:23], a[68:71], a[112:115], v[20:23]\n\t"
    "ds_read_b128 a[40:43], %[vb] offset:512\n\t"
    "v_mfma_f32_16x16x32_bf16 v[20:23], a[72:75], a[108:111], v[20:23]\n\t"
    "ds_read_b64 a[20:21], %[va] offset:10752\n\t"
    "v_mfma_f32_16x16x32_bf16 v[20:23], a[76:79], a[104:107], v[20:23]\n\t"
    "ds_read_b64 a[22:23], %[va] offset:10784\n\t"
    "v_mfma_f32_16x16x32_bf16 v[20:23], a[80:83], a[100:103], v[20:23]\n\t"
    "ds_read_b128 a[36:39], %[vb] offset:256\n\t"
    "v_mfma_f32_16x16x32_bf16 v[20:23], a[84:87], a[96:99], v[20:23]\n\t"
    "ds_read_b64 a[24:25], %[va] offset:12800\n\t"
    "v_mfma_f32_16x16x32_bf16 v[16:19], a[64:67], a[112:115], v[16:19]\n\t"
    "ds_read_b64 a[26:27], %[va] offset:12832\n\t"
    "v_mfma_f32_16x16x32_bf16 v[16:19], a[68:71], a[108:111], v[16:19]\n\t"
    "ds_read_b128 a[32:35], %[vb] offset:0\n\t"
    "v_mfma_f32_16x16x32_bf16 v[16:19], a[72:75], a[104:107], v[16:19]\n\t"
    "ds_read_b64 a[28:29], %[va] offset:14848\n\t"
    "v_mfma_f32_16x16x32_bf16 v[16:19], a[76:79], a[100:103], v[16:19]\n\t"
    "ds_read_b64 a[30:31], %[va] offset:14880\n\t"
    "v_mfma_f32_16x16x32_bf16 v[16:19], a[80:83], a[96:99], v[16:19]\n\t"
    "v_mfma_f32_16x16x32_bf16 v[12:15], a[64:67], a[108:111], v[12:15]\n\t"
    "v_mfma_f32_16x16x32_bf16 v[12:15], a[68:71], a[104:107], v[12:15]\n\t"
    "v_mfma_f32_16x16x32_bf16 v[12:15], a[72:75], a[100:103], v[12:15]\n\t"
    "v_mfma_f32_16x16x32_bf16 v[12:15], a[76:79], a[96:99], v[12:15]\n\t"
    "v_mfma_f32_16x16x32_bf16 v[8:11], a[64:67], a[104:107], v[8:11]\n\t"
    "v_mfma_f32_16x16x32_bf16 v[8:11], a[68:71], a[100:103], v[8:11]\n\t"
    "v_mfma_f32_16x16x32_bf16 v[8:11], a[72:75], a[96:99], v[8:11]\n\t"
    "v_mfma_f32_16x16x32_bf16 v[4:7], a[64:67], a[100:103], v[4:7]\n\t"
    "v_mfma_f32_16x16x32_bf16 v[4:7], a[68:71], a[96:99], v[4:7]\n\t"
    "v_mfma_f32_16x16x32_bf16 v[0:3], a[64:67], a[96:99], v[0:3]\n\t"
    "v_add_u32 %[va], 512, %[va]\n\t"
    "v_subrev_u32 %[vb], 64, %[vb]\n\t"
    "s_sub_u32 s45, s45, 1\n\t"
    "s_cmp_lg_u32 s45, 0\n\t"
    "s_cbranch_scc1 L_cv_loop_%=\n\t"
    "s_waitcnt lgkmcnt(0)\n\t"
    "s_nop 7\n\t"
        : "=&{v[0:15]}"(aA), "=&{v[16:31]}"(aB), [va] "+v"(va), [vb] "+v"(vb) :: "memory", "scc", "a0", "a1", "a2", "a3", "a4", "a5", "a6", "a7", "a8", "a9", "a10", "a11", "a12", "a13", "a14", "a15", "a16", "a17", "a18", "a19", "a20", "a21", "a22", "a23", "a24", "a25", "a26", "a27", "a28", "a29", "a30", "a31", "a32", "a33", "a34", "a35", "a36", "a37", "a38", "a39", "a40", "a41", "a42", "a43", "a44", "a45", "a46", "a47", "a48", "a49", "a50", "a51", "a52", "a53", "a54", "a55", "a56", "a57", "a58", "a59", "a60", "a61", "a62", "a63", "a64", "a65", "a66", "a67", "a68", "a69", "a70", "a71", "a72", "a73", "a74", "a75", "a76", "a77", "a78", "a79", "a80", "a81", "a82", "a83", "a84", "a85", "a86", "a87", "a88", "a89", "a90", "a91", "a92", "a93", "a94", "a95", "a96", "a97", "a98", "a99", "a100", "a101", "a102", "a103", "a104", "a105", "a106", "a107", "a108", "a109", "a110", "a111", "a112", "a113", "a114", "a115", "a116", "a117", "a118", "a119", "a120", "a121", "a122", "a123", "a124", "a125", "a126", "a127", "s45");
    }
    char* Z = lds + CV_Z0;
    {
      u32x2 kvp[8];
#pragma unroll
      for (int t = 0; t < 8; t++) {
        const int uu = 32 * t + 2 * lb + (g >> 1);
        kvp[t] = *(const u32x2*)(lds + CV_X0 + c * CV_XCH + (uu & 1) * CV_XO + ((uu >> 1) + 16) * 16 + ((g & 1) ? 0 : 8));
      }
      u32x2 o[8];
#pragma unroll
      for (int t = 0; t < 8; t++) {
        const float y0 = t < 4 ? aA[4 * t] : aB[4 * t - 16], y1 = t < 4 ? aA[4 * t + 1] : aB[4 * t - 15], y2 = t < 4 ? aA[4 * t + 2] : aB[4 * t - 14], y3 = t < 4 ? aA[4 * t + 3] : aB[4 * t - 13];
        o[t] = (u32x2){pk2(fmaf(bfhi(kvp[t][1]), dd, y0), fmaf(bflo(kvp[t][1]), dd, y1)), pk2(fmaf(bfhi(kvp[t][0]), dd, y2), fmaf(bflo(kvp[t][0]), dd, y3))};
      }
#pragma unroll
      for (int t = 0; t < 8; t++) *(u32x2*)(Z + c * 4112 + (256 * t + 16 * lb + 4 * g) * 2) = o[t];
    }
    asm volatile("s_waitcnt lgkmcnt(0)\n\ts_barrier" ::: "memory");
    {
      u32x4 o[4];
#pragma unroll
      for (int i = 0; i < 4; i++) { const int idx = tid + 256 * i; o[i] = *(const u32x4*)(Z + (idx & 3) * 4112 + (idx >> 2) * 16); }
#pragma unroll
      for (int i = 0; i < 4; i++) { const int idx = tid + 256 * i; st16_dev(P.zB + ((size_t)(idx >> 2) * D_ + c0 + (idx & 3)) * 8, o[i]); }
    }
    drain_and_barrier();
    arrive(P.flags + FL_B2 + (b & 15) * 32);
  }
  const int unit = xcd * 28 + li, pn1 = unit >> 3, pm1 = unit & 7;
  const int g = lane >> 4, qq = (lane & 15) >> 2, p4 = lane & 3;
  const u32 dsw0 = 16 * ((((lane & 15) >> 1) ^ (lane >> 4)) * 2 + (lane & 1)), dsw1 = 16 * ((((lane & 15) >> 1) ^ ((lane >> 4) + 4)) * 2 + (lane & 1));
  {
    float* red = (float*)(lds + LN_RED);
    float piv[8], s1[8], s2[8], mb[8], rs[8];
    f32x2 xa[8], xb[8], qa[8], qb[8];
#pragma unroll
    for (int r = 0; r < 8; r++) piv[r] = __int_as_float(__builtin_amdgcn_readlane(__float_as_int(pvl), r));
    auto sums = [&](int r) {
      const f32x2 pp = {piv[r], piv[r]};
      const f32x2 da = xa[r] - pp, db = xb[r] - pp;
      const f32x2 t1 = da + db, t2 = __builtin_elementwise_fma(db, db, da * da);
      s1[r] = t1[0] + t1[1]; s2[r] = t2[0] + t2[1];
    };
#pragma unroll
    for (int r = 0; r < 8; r++) {
      xa[r] = (f32x2){bflo(xx[r][0]), bfhi(xx[r][0])}; xb[r] = (f32x2){bflo(xx[r][1]), bfhi(xx[r][1])};
      sums(r);
    }
    row_stats(s1, s2, pvl, red, w, lane, mb, rs);
#pragma unroll
    for (int r = 0; r < 8; r++) {
      const f32x2 rr = {rs[r], rs[r]}, cc = rr * -mb[r];
      qa[r] = __builtin_elementwise_fma(xa[r], rr, cc); qb[r] = __builtin_elementwise_fma(xb[r], rr, cc);
    }
    wait_counters(P.flags + FL_B2, 16, 32, 16u);
    u32x4 z0, z1, z2, z3;
    const char* zp = (const char*)P.zB + ((size_t)u * D_ + 4 * tid) * 16;
    if (li < 28) {
      const u32 drow = (u32)((32 * (w & 1) + (lane >> 4)) * 7168 + pn1 * 256);
      const u32 vdma0 = drow + dsw0, vdma1 = vdma0 + 27648, vdma2 = drow + dsw1 + 2 * 27648, vdma3 = vdma2 + 27648;
      const __amdgpu_buffer_rsrc_t sw = __builtin_amdgcn_make_buffer_rsrc((void*)(w < 2 ? P.wg : P.wu), (short)0, D_ * F_ * 2, 0x00020000);
      const u32 sm0 = rfl(ldsb + (w >> 1) * 16384 + (w & 1) * 8192);
      asm volatile(
        "global_load_dwordx4 %0, %4, off sc1\n\tglobal_load_dwordx4 %1, %4, off offset:16 sc1\n\tglobal_load_dwordx4 %2, %4, off offset:32 sc1\n\tglobal_load_dwordx4 %3, %4, off offset:48 sc1\n\t"
    "s_mov_b32 s42, 0\n\t"
    "s_add_u32 m0, %[sm0], 0\n\t"
    "s_add_u32 s43, s42, 0\n\t"
    "buffer_load_dwordx4 %[vdma0], %[sw], s43 offen offset:0 nt lds\n\t"
    "buffer_load_dwordx4 %[vdma1], %[sw], s43 offen offset:1024 nt lds\n\t"
    "buffer_load_dwordx4 %[vdma2], %[sw], s43 offen offset:2048 nt lds\n\t"
    "buffer_load_dwordx4 %[vdma3], %[sw], s43 offen offset:3072 nt lds\n\t"
    "s_add_u32 m0, %[sm0], 4096\n\t"
    "s_add_u32 s43, s42, 114688\n\t"
    "buffer_load_dwordx4 %[vdma0], %[sw], s43 offen offset:0 nt lds\n\t"
    "buffer_load_dwordx4 %[vdma1], %[sw], s43 offen offset:1024 nt lds\n\t"
    "buffer_load_dwordx4 %[vdma2], %[sw], s43 offen offset:2048 nt lds\n\t"
    "buffer_load_dwordx4 %[vdma3], %[sw], s43 offen offset:3072 nt lds\n\t"
    "s_add_u32 s42, s42, 458752\n\t"
        "s_waitcnt vmcnt(8)"
        : "=&v"(z0), "=&v"(z1), "=&v"(z2), "=&v"(z3) : "v"(zp), [vdma0] "v"(vdma0), [vdma1] "v"(vdma1), [vdma2] "v"(vdma2), [vdma3] "v"(vdma3), [sw] "s"(sw), [sm0] "s"(sm0) : "memory", "scc", "s42", "s43");
    } else {
      asm volatile("global_load_dwordx4 %0, %4, off sc1\n\tglobal_load_dwordx4 %1, %4, off offset:16 sc1\n\tglobal_load_dwordx4 %2, %4, off offset:32 sc1\n\tglobal_load_dwordx4 %3, %4, off offset:48 sc1\n\ts_waitcnt vmcnt(0)"
                   : "=&v"(z0), "=&v"(z1), "=&v"(z2), "=&v"(z3) : "v"(zp) : "memory");
    }
    char* smh = lds + LN_H0;
#pragma unroll
    for (int r = 0; r < 8; r++) {
      const f32x2 za = (r & 1) ? (f32x2){bfhi(z0[r >> 1]), bfhi(z1[r >> 1])} : (f32x2){bflo(z0[r >> 1]), bflo(z1[r >> 1])};
      const f32x2 zb = (r & 1) ? (f32x2){bfhi(z2[r >> 1]), bfhi(z3[r >> 1])} : (f32x2){bflo(z2[r >> 1]), bflo(z3[r >> 1])};
      const f32x2 ha = __builtin_elementwise_fma(qa[r], za, xa[r]), hb2 = __builtin_elementwise_fma(qb[r], zb, xb[r]);
      const u32x2 hb = {pk2(ha[0], ha[1]), pk2(hb2[0], hb2[1])};
      *(u32x2*)(smh + r * 2048 + tid * 8) = hb;
      xa[r] = (f32x2){bflo(hb[0]), bfhi(hb[0])}; xb[r] = (f32x2){bflo(hb[1]), bfhi(hb[1])};
      sums(r);
    }
    row_stats(s1, s2, pvl, red + 64, w, lane, mb, rs);
    {
      u32x4 o[4];
#pragma unroll
      for (int i = 0; i < 4; i++) o[i] = *(const u32x4*)(smh + (tid + 256 * i) * 16);
#pragma unroll
      for (int i = 0; i < 4; i++) st16_dev((char*)P.h + (size_t)u * 16384 + (tid + 256 * i) * 16, o[i]);
    }
    char* sm = lds + LN_S0;
#pragma unroll
    for (int r = 0; r < 8; r++) {
      const f32x2 rr = {rs[r], rs[r]}, cc = rr * -mb[r];
      const f32x2 oa = __builtin_elementwise_fma(xa[r], rr, cc), ob = __builtin_elementwise_fma(xb[r], rr, cc);
      *(u32x2*)(sm + r * 2064 + tid * 8) = (u32x2){pk2(oa[0], oa[1]), pk2(ob[0], ob[1])};
    }
    asm volatile("s_waitcnt lgkmcnt(0)\n\ts_barrier" ::: "memory");
    {
      u32x4 o[4];
#pragma unroll
      for (int i = 0; i < 4; i++) { const int idx = tid + 256 * i; o[i] = *(const u32x4*)(sm + (idx & 7) * 2064 + (idx >> 3) * 16); }
#pragma unroll
      for (int i = 0; i < 4; i++) {
        const int idx = tid + 256 * i, G = idx >> 3, token = u * 8 + (idx & 7);
        st16_dev((char*)P.hn + ((size_t)((G >> 2) * 128 + (token >> 4)) * 1024 + (G & 3) * 256 + (token & 15) * 16), o[i]);
      }
    }
    drain_and_barrier();
    arrive(P.flags + FL_B3 + ((u >> 5) * 4 + (u & 3)) * 32);
  }
  if (li < 28 && w != 0) {
#pragma unroll 1
    for (int ww = (w == 1 ? 0 : w); ww <= w; ww++) {
      const u32 drow = (u32)((32 * (ww & 1) + (lane >> 4)) * 7168 + pn1 * 256);
      const u32 vdma0 = drow + dsw0, vdma1 = vdma0 + 27648, vdma2 = drow + dsw1 + 2 * 27648, vdma3 = vdma2 + 27648;
      const __amdgpu_buffer_rsrc_t sw = __builtin_amdgcn_make_buffer_rsrc((void*)(w < 2 ? P.wg : P.wu), (short)0, D_ * F_ * 2, 0x00020000);
      const u32 sm0 = rfl(ldsb + (ww >> 1) * 16384 + (ww & 1) * 8192);
      asm volatile(
    "s_mov_b32 s42, 458752\n\t"
    "s_add_u32 m0, %[sm0], 32768\n\t"
    "s_add_u32 s43, s42, 0\n\t"
    "buffer_load_dwordx4 %[vdma0], %[sw], s43 offen offset:0 nt lds\n\t"
    "buffer_load_dwordx4 %[vdma1], %[sw], s43 offen offset:1024 nt lds\n\t"
    "buffer_load_dwordx4 %[vdma2], %[sw], s43 offen offset:2048 nt lds\n\t"
    "buffer_load_dwordx4 %[vdma3], %[sw], s43 offen offset:3072 nt lds\n\t"
    "s_add_u32 m0, %[sm0], 36864\n\t"
    "s_add_u32 s43, s42, 114688\n\t"
    "buffer_load_dwordx4 %[vdma0], %[sw], s43 offen offset:0 nt lds\n\t"
    "buffer_load_dwordx4 %[vdma1], %[sw], s43 offen offset:1024 nt lds\n\t"
    "buffer_load_dwordx4 %[vdma2], %[sw], s43 offen offset:2048 nt lds\n\t"
    "buffer_load_dwordx4 %[vdma3], %[sw], s43 offen offset:3072 nt lds\n\t"
    "s_add_u32 s42, s42, 458752\n\t"
    "s_add_u32 m0, %[sm0], 65536\n\t"
    "s_add_u32 s43, s42, 0\n\t"
    "buffer_load_dwordx4 %[vdma0], %[sw], s43 offen offset:0 nt lds\n\t"
    "buffer_load_dwordx4 %[vdma1], %[sw], s43 offen offset:1024 nt lds\n\t"
    "buffer_load_dwordx4 %[vdma2], %[sw], s43 offen offset:2048 nt lds\n\t"
    "buffer_load_dwordx4 %[vdma3], %[sw], s43 offen offset:3072 nt lds\n\t"
    "s_add_u32 m0, %[sm0], 69632\n\t"
    "s_add_u32 s43, s42, 114688\n\t"
    "buffer_load_dwordx4 %[vdma0], %[sw], s43 offen offset:0 nt lds\n\t"
    "buffer_load_dwordx4 %[vdma1], %[sw], s43 offen offset:1024 nt lds\n\t"
    "buffer_load_dwordx4 %[vdma2], %[sw], s43 offen offset:2048 nt lds\n\t"
    "buffer_load_dwordx4 %[vdma3], %[sw], s43 offen offset:3072 nt lds\n\t"
    "s_add_u32 s42, s42, 458752\n\t"
        :: [vdma0] "v"(vdma0), [vdma1] "v"(vdma1), [vdma2] "v"(vdma2), [vdma3] "v"(vdma3), [sw] "s"(sw), [sm0] "s"(sm0) : "memory", "scc", "s42", "s43");
    }
  }
  if (li < 28) {
    const u32* flag = P.flags + FL_B3 + pm1 * 128; const u32 want = 8u, vpoll = (lane & 3) * 128;
    const int t16 = pm1 * 16 + 4 * w;
    const u32 vtrA = ldsb + g * 2048 + qq * 256 + 8 * p4 + 32 * (qq + 4 * (g & 1));
    const u32 vtrB = vtrA + 65536;
    const u32 vtok = lane * 16 + t16 * 1024;
    const u32 drow = (u32)((32 * (w & 1) + (lane >> 4)) * 7168 + pn1 * 256);
    const u32 vdma0 = drow + dsw0, vdma1 = vdma0 + 27648, vdma2 = drow + dsw1 + 2 * 27648, vdma3 = vdma2 + 27648;
    const __amdgpu_buffer_rsrc_t stok = __builtin_amdgcn_make_buffer_rsrc((void*)P.hn, (short)0, S_ * D_ * 2, 0x00020000);
    const __amdgpu_buffer_rsrc_t sw = __builtin_amdgcn_make_buffer_rsrc((void*)(w < 2 ? P.wg : P.wu), (short)0, D_ * F_ * 2, 0x00020000);
    const u32 sm0 = rfl(ldsb + (w >> 1) * 16384 + (w & 1) * 8192);
    f32x16 a0, a1, a2, a3, a4, a5, a6, a7, a8, a9, a10, a11, a12, a13, a14, a15; u32 vt, vt2;
    asm volatile(
    "s_mov_b32 s50, 0\n\t"
    "s_mov_b32 s51, 131072\n\t"
    "s_mov_b32 s42, 1376256\n\t"
    "v_accvgpr_write_b32 a0, 0\n\t"
    "v_accvgpr_write_b32 a1, 0\n\t"
    "v_accvgpr_write_b32 a2, 0\n\t"
    "v_accvgpr_write_b32 a3, 0\n\t"
    "s_nop 1\n\t"
    "v_mfma_f32_32x32x16_bf16 v[0:15], a[0:3], a[0:3], 0\n\t"
    "v_mfma_f32_32x32x16_bf16 v[16:31], a[0:3], a[0:3], 0\n\t"
    "v_mfma_f32_32x32x16_bf16 v[32:47], a[0:3], a[0:3], 0\n\t"
    "v_mfma_f32_32x32x16_bf16 v[48:63], a[0:3], a[0:3], 0\n\t"
    "v_mfma_f32_32x32x16_bf16 v[64:79], a[0:3], a[0:3], 0\n\t"
    "v_mfma_f32_32x32x16_bf16 v[80:95], a[0:3], a[0:3], 0\n\t"
    "v_mfma_f32_32x32x16_bf16 v[96:111], a[0:3], a[0:3], 0\n\t"
    "v_mfma_f32_32x32x16_bf16 v[112:127], a[0:3], a[0:3], 0\n\t"
    "v_mfma_f32_32x32x16_bf16 v[128:143], a[0:3], a[0:3], 0\n\t"
    "v_mfma_f32_32x32x16_bf16 v[144:159], a[0:3], a[0:3], 0\n\t"
    "v_mfma_f32_32x32x16_bf16 v[160:175], a[0:3], a[0:3], 0\n\t"
    "v_mfma_f32_32x32x16_bf16 v[176:191], a[0:3], a[0:3], 0\n\t"
    "v_mfma_f32_32x32x16_bf16 v[192:207], a[0:3], a[0:3], 0\n\t"
    "v_mfma_f32_32x32x16_bf16 v[208:223], a[0:3], a[0:3], 0\n\t"
    "v_mfma_f32_32x32x16_bf16 v[224:239], a[0:3], a[0:3], 0\n\t"
    "v_mfma_f32_32x32x16_bf16 a[160:175], a[0:3], a[0:3], 0\n\t"
    "s_cmp_lg_u32 %[wid], 0\n\t"
    "s_cbranch_scc1 L_g1_go_%=\n\t"
    "s_mov_b32 s44, 0x20000\n\t"
    "L_g1_poll_%=:\n\t"
    "global_load_dword %[vt2], %[vpoll], %[flag] sc1\n\t"
    "s_waitcnt vmcnt(0)\n\t"
    "v_cmp_gt_u32 vcc, %[want], %[vt2]\n\t"
    "s_cbranch_vccz L_g1_go_%=\n\t"
    "s_sleep 1\n\t"
    "s_sub_u32 s44, s44, 1\n\t"
    "s_cmp_lg_u32 s44, 0\n\t"
    "s_cbranch_scc1 L_g1_poll_%=\n\t"
    "L_g1_go_%=:\n\t"
    "s_barrier\n\t"
    "buffer_load_dwordx4 a[32:35], %[vtok], %[stok], s50 offen offset:0 sc1\n\t"
    "buffer_load_dwordx4 a[36:39], %[vtok], %[stok], s51 offen offset:0 sc1\n\t"
    "buffer_load_dwordx4 a[40:43], %[vtok], %[stok], s50 offen offset:1024 sc1\n\t"
    "buffer_load_dwordx4 a[44:47], %[vtok], %[stok], s51 offen offset:1024 sc1\n\t"
    "buffer_load_dwordx4 a[48:51], %[vtok], %[stok], s50 offen offset:2048 sc1\n\t"
    "buffer_load_dwordx4 a[52:55], %[vtok], %[stok], s51 offen offset:2048 sc1\n\t"
    "buffer_load_dwordx4 a[56:59], %[vtok], %[stok], s50 offen offset:3072 sc1\n\t"
    "buffer_load_dwordx4 a[60:63], %[vtok], %[stok], s51 offen offset:3072 sc1\n\t"
    "s_mov_b32 s54, 262144\n\t"
    "s_mov_b32 s55, 393216\n\t"
    "s_mov_b32 s56, 524288\n\t"
    "s_mov_b32 s57, 655360\n\t"
    "buffer_load_dwordx4 a[64:67], %[vtok], %[stok], s54 offen offset:0 sc1\n\t"
    "buffer_load_dwordx4 a[68:71], %[vtok], %[stok], s55 offen offset:0 sc1\n\t"
    "buffer_load_dwordx4 a[72:75], %[vtok], %[stok], s54 offen offset:1024 sc1\n\t"
    "buffer_load_dwordx4 a[76:79], %[vtok], %[stok], s55 offen offset:1024 sc1\n\t"
    "buffer_load_dwordx4 a[80:83], %[vtok], %[stok], s54 offen offset:2048 sc1\n\t"
    "buffer_load_dwordx4 a[84:87], %[vtok], %[stok], s55 offen offset:2048 sc1\n\t"
    "buffer_load_dwordx4 a[88:91], %[vtok], %[stok], s54 offen offset:3072 sc1\n\t"
    "buffer_load_dwordx4 a[92:95], %[vtok], %[stok], s55 offen offset:3072 sc1\n\t"
    "s_add_u32 s50, s50, 262144\n\t"
    "s_add_u32 s51, s51, 262144\n\t"
    "s_add_u32 s50, s50, 262144\n\t"
    "s_add_u32 s51, s51, 262144\n\t"
    "s_add_u32 s50, s50, 262144\n\t"
    "s_add_u32 s51, s51, 262144\n\t"
    "s_mov_b32 s45, 4\n\t"
    "s_waitcnt vmcnt(8)\n\t"
    "s_barrier\n\t"
    "ds_read_b64_tr_b16 a[0:1], %[vtrA] offset:0\n\t"
    "ds_read_b64_tr_b16 a[2:3], %[vtrA] offset:1024\n\t"
    "ds_read_b64_tr_b16 a[4:5], %[vtrA] offset:8192\n\t"
    "ds_read_b64_tr_b16 a[6:7], %[vtrA] offset:9216\n\t"
    "v_xor_b32 %[vt], 32, %[vtrA]\n\t"
    "ds_read_b64_tr_b16 a[8:9], %[vt] offset:0\n\t"
    "ds_read_b64_tr_b16 a[10:11], %[vt] offset:1024\n\t"
    "ds_read_b64_tr_b16 a[12:13], %[vt] offset:8192\n\t"
    "ds_read_b64_tr_b16 a[14:15], %[vt] offset:9216\n\t"
    "s_add_u32 m0, %[sm0], 98304\n\t"
    "s_add_u32 s43, s42, 0\n\t"
    "buffer_load_dwordx4 %[vdma0], %[sw], s43 offen offset:0 nt lds\n\t"
    "buffer_load_dwordx4 %[vdma1], %[sw], s43 offen offset:1024 nt lds\n\t"
    "L_g1_loop_%=:\n\t"
    "s_waitcnt lgkmcnt(4)\n\t"
    "v_mfma_f32_16x16x32_bf16 v[0:3], a[0:3], a[32:35], v[0:3]\n\t"
    "s_cmp_lg_u32 s45, 4\n\t"
    "s_cbranch_scc1 L_g1_x0_%=\n\t"
    "buffer_load_dwordx4 a[96:99], %[vtok], %[stok], s56 offen offset:0 sc1\n\t"
    "L_g1_x0_%=:\n\t"
    "v_mfma_f32_16x16x32_bf16 v[0:3], a[4:7], a[36:39], v[0:3]\n\t"
    "v_xor_b32 %[vt], 64, %[vtrA]\n\t"
    "v_mfma_f32_16x16x32_bf16 v[4:7], a[0:3], a[40:43], v[4:7]\n\t"
    "buffer_load_dwordx4 a[128:131], %[vtok], %[stok], s50 offen offset:0 sc1\n\t"
    "v_mfma_f32_16x16x32_bf16 v[4:7], a[4:7], a[44:47], v[4:7]\n\t"
    "ds_read_b64_tr_b16 a[16:17], %[vt] offset:0\n\t"
    "v_mfma_f32_16x16x32_bf16 v[8:11], a[0:3], a[48:51], v[8:11]\n\t"
    "ds_read_b64_tr_b16 a[18:19], %[vt] offset:1024\n\t"
    "v_mfma_f32_16x16x32_bf16 v[8:11], a[4:7], a[52:55], v[8:11]\n\t"
    "ds_read_b64_tr_b16 a[20:21], %[vt] offset:8192\n\t"
    "v_mfma_f32_16x16x32_bf16 v[12:15], a[0:3], a[56:59], v[12:15]\n\t"
    "ds_read_b64_tr_b16 a[22:23], %[vt] offset:9216\n\t"
    "v_mfma_f32_16x16x32_bf16 v[12:15], a[4:7], a[60:63], v[12:15]\n\t"
    "s_waitcnt lgkmcnt(4)\n\t"
    "v_mfma_f32_16x16x32_bf16 v[16:19], a[8:11], a[32:35], v[16:19]\n\t"
    "s_cmp_lg_u32 s45, 4\n\t"
    "s_cbranch_scc1 L_g1_x1_%=\n\t"
    "buffer_load_dwordx4 a[100:103], %[vtok], %[stok], s57 offen offset:0 sc1\n\t"
    "L_g1_x1_%=:\n\t"
    "v_mfma_f32_16x16x32_bf16 v[16:19], a[12:15], a[36:39], v[16:19]\n\t"
    "v_xor_b32 %[vt], 96, %[vtrA]\n\t"
    "v_mfma_f32_16x16x32_bf16 v[20:23], a[8:11], a[40:43], v[20:23]\n\t"
    "buffer_load_dwordx4 %[vdma2], %[sw], s43 offen offset:2048 nt lds\n\t"
    "v_mfma_f32_16x16x32_bf16 v[20:23], a[12:15], a[44:47], v[20:23]\n\t"
    "ds_read_b64_tr_b16 a[24:25], %[vt] offset:0\n\t"
    "v_mfma_f32_16x16x32_bf16 v[24:27], a[8:11], a[48:51], v[24:27]\n\t"
    "ds_read_b64_tr_b16 a[26:27], %[vt] offset:1024\n\t"
    "v_mfma_f32_16x16x32_bf16 v[24:27], a[12:15], a[52:55], v[24:27]\n\t"
    "ds_read_b64_tr_b16 a[28:29], %[vt] offset:8192\n\t"
    "v_mfma_f32_16x16x32_bf16 v[28:31], a[8:11], a[56:59], v[28:31]\n\t"
    "ds_read_b64_tr_b16 a[30:31], %[vt] offset:9216\n\t"
    "v_mfma_f32_16x16x32_bf16 v[28:31], a[12:15], a[60:63], v[28:31]\n\t"
    "s_waitcnt lgkmcnt(4)\n\t"
    "v_mfma_f32_16x16x32_bf16 v[32:35], a[16:19], a[32:35], v[32:35]\n\t"
    "s_cmp_lg_u32 s45, 4\n\t"
    "s_cbranch_scc1 L_g1_x2_%=\n\t"
    "buffer_load_dwordx4 a[104:107], %[vtok], %[stok], s56 offen offset:1024 sc1\n\t"
    "L_g1_x2_%=:\n\t"
    "v_mfma_f32_16x16x32_bf16 v[32:35], a[20:23], a[36:39], v[32:35]\n\t"
    "v_xor_b32 %[vt], 128, %[vtrA]\n\t"
    "v_mfma_f32_16x16x32_bf16 v[36:39], a[16:19], a[40:43], v[36:39]\n\t"
    "buffer_load_dwordx4 a[132:135], %[vtok], %[stok], s51 offen offset:0 sc1\n\t"
    "v_mfma_f32_16x16x32_bf16 v[36:39], a[20:23], a[44:47], v[36:39]\n\t"
    "ds_read_b64_tr_b16 a[0:1], %[vt] offset:0\n\t"
    "v_mfma_f32_16x16x32_bf16 v[40:43], a[16:19], a[48:51], v[40:43]\n\t"
    "ds_read_b64_tr_b16 a[2:3], %[vt] offset:1024\n\t"
    "v_mfma_f32_16x16x32_bf16 v[40:43], a[20:23], a[52:55], v[40:43]\n\t"
    "ds_read_b64_tr_b16 a[4:5], %[vt] offset:8192\n\t"
    "v_mfma_f32_16x16x32_bf16 v[44:47], a[16:19], a[56:59], v[44:47]\n\t"
    "ds_read_b64_tr_b16 a[6:7], %[vt] offset:9216\n\t"
    "v_mfma_f32_16x16x32_bf16 v[44:47], a[20:23], a[60:63], v[44:47]\n\t"
    "s_waitcnt lgkmcnt(4)\n\t"
    "v_mfma_f32_16x16x32_bf16 v[48:51], a[24:27], a[32:35], v[48:51]\n\t"
    "s_cmp_lg_u32 s45, 4\n\t"
    "s_cbranch_scc1 L_g1_x3_%=\n\t"
    "buffer_load_dwordx4 a[108:111], %[vtok], %[stok], s57 offen offset:1024 sc1\n\t"
    "L_g1_x3_%=:\n\t"
    "v_mfma_f32_16x16x32_bf16 v[48:51], a[28:31], a[36:39], v[48:51]\n\t"
    "v_xor_b32 %[vt], 160, %[vtrA]\n\t"
    "v_mfma_f32_16x16x32_bf16 v[52:55], a[24:27], a[40:43], v[52:55]\n\t"
    "buffer_load_dwordx4 %[vdma3], %[sw], s43 offen offset:3072 nt lds\n\t"
    "v_mfma_f32_16x16x32_bf16 v[52:55], a[28:31], a[44:47], v[52:55]\n\t"
    "ds_read_b64_tr_b16 a[8:9], %[vt] offset:0\n\t"
    "v_mfma_f32_16x16x32_bf16 v[56:59], a[24:27], a[48:51], v[56:59]\n\t"
    "ds_read_b64_tr_b16 a[10:11], %[vt] offset:1024\n\t"
    "v_mfma_f32_16x16x32_bf16 v[56:59], a[28:31], a[52:55], v[56:59]\n\t"
    "ds_read_b64_tr_b16 a[12:13], %[vt] offset:8192\n\t"
    "v_mfma_f32_16x16x32_bf16 v[60:63], a[24:27], a[56:59], v[60:63]\n\t"
    "ds_read_b64_tr_b16 a[14:15], %[vt] offset:9216\n\t"
    "v_mfma_f32_16x16x32_bf16 v[60:63], a[28:31], a[60:63], v[60:63]\n\t"
    "s_waitcnt lgkmcnt(4)\n\t"
    "v_mfma_f32_16x16x32_bf16 v[64:67], a[0:3], a[32:35], v[64:67]\n\t"
    "s_cmp_lg_u32 s45, 4\n\t"
    "s_cbranch_scc1 L_g1_x4_%=\n\t"
    "buffer_load_dwordx4 a[112:115], %[vtok], %[stok], s56 offen offset:2048 sc1\n\t"
    "L_g1_x4_%=:\n\t"
    "v_mfma_f32_16x16x32_bf16 v[64:67], a[4:7], a[36:39], v[64:67]\n\t"
    "v_xor_b32 %[vt], 192, %[vtrA]\n\t"
    "v_mfma_f32_16x16x32_bf16 v[68:71], a[0:3], a[40:43], v[68:71]\n\t"
    "buffer_load_dwordx4 a[136:139], %[vtok], %[stok], s50 offen offset:1024 sc1\n\t"
    "v_mfma_f32_16x16x32_bf16 v[68:71], a[4:7], a[44:47], v[68:71]\n\t"
    "ds_read_b64_tr_b16 a[16:17], %[vt] offset:0\n\t"
    "v_mfma_f32_16x16x32_bf16 v[72:75], a[0:3], a[48:51], v[72:75]\n\t"
    "ds_read_b64_tr_b16 a[18:19], %[vt] offset:1024\n\t"
    "v_mfma_f32_16x16x32_bf16 v[72:75], a[4:7], a[52:55], v[72:75]\n\t"
    "ds_read_b64_tr_b16 a[20:21], %[vt] offset:8192\n\t"
    "v_mfma_f32_16x16x32_bf16 v[76:79], a[0:3], a[56:59], v[76:79]\n\t"
    "ds_read_b64_tr_b16 a[22:23], %[vt] offset:9216\n\t"
    "v_mfma_f32_16x16x32_bf16 v[76:79], a[4:7], a[60:63], v[76:79]\n\t"
    "s_waitcnt lgkmcnt(4)\n\t"
    "v_mfma_f32_16x16x32_bf16 v[80:83], a[8:11], a[32:35], v[80:83]\n\t"
    "s_cmp_lg_u32 s45, 4\n\t"
    "s_cbranch_scc1 L_g1_x5_%=\n\t"
    "buffer_load_dwordx4 a[116:119], %[vtok], %[stok], s57 offen offset:2048 sc1\n\t"
    "L_g1_x5_%=:\n\t"
    "v_mfma_f32_16x16x32_bf16 v[80:83], a[12:15], a[36:39], v[80:83]\n\t"
    "v_xor_b32 %[vt], 224, %[vtrA]\n\t"
    "v_mfma_f32_16x16x32_bf16 v[84:87], a[8:11], a[40:43], v[84:87]\n\t"
    "s_add_u32 m0, %[sm0], 102400\n\t"
    "s_add_u32 s43, s42, 114688\n\t"
    "buffer_load_dwordx4 %[vdma0], %[sw], s43 offen offset:0 nt lds\n\t"
    "v_mfma_f32_16x16x32_bf16 v[84:87], a[12:15], a[44:47], v[84:87]\n\t"
    "ds_read_b64_tr_b16 a[24:25], %[vt] offset:0\n\t"
    "v_mfma_f32_16x16x32_bf16 v[88:91], a[8:11], a[48:51], v[88:91]\n\t"
    "ds_read_b64_tr_b16 a[26:27], %[vt] offset:1024\n\t"
    "v_mfma_f32_16x16x32_bf16 v[88:91], a[12:15], a[52:55], v[88:91]\n\t"
    "ds_read_b64_tr_b16 a[28:29], %[vt] offset:8192\n\t"
    "v_mfma_f32_16x16x32_bf16 v[92:95], a[8:11], a[56:59], v[92:95]\n\t"
    "ds_read_b64_tr_b16 a[30:31], %[vt] offset:9216\n\t"
    "v_mfma_f32_16x16x32_bf16 v[92:95], a[12:15], a[60:63], v[92:95]\n\t"
    "s_waitcnt lgkmcnt(4)\n\t"
    "v_mfma_f32_16x16x32_bf16 v[96:99], a[16:19], a[32:35], v[96:99]\n\t"
    "s_cmp_lg_u32 s45, 4\n\t"
    "s_cbranch_scc1 L_g1_x6_%=\n\t"
    "buffer_load_dwordx4 a[120:123], %[vtok], %[stok], s56 offen offset:3072 sc1\n\t"
    "L_g1_x6_%=:\n\t"
    "v_mfma_f32_16x16x32_bf16 v[96:99], a[20:23], a[36:39], v[96:99]\n\t"
    "ds_read_b64_tr_b16 a[0:1], %[vtrA] offset:16384\n\t"
    "v_mfma_f32_16x16x32_bf16 v[100:103], a[16:19], a[40:43], v[100:103]\n\t"
    "buffer_load_dwordx4 a[140:143], %[vtok], %[stok], s51 offen offset:1024 sc1\n\t"
    "v_mfma_f32_16x16x32_bf16 v[100:103], a[20:23], a[44:47], v[100:103]\n\t"
    "v_mfma_f32_16x16x32_bf16 v[104:107], a[16:19], a[48:51], v[104:107]\n\t"
    "ds_read_b64_tr_b16 a[2:3], %[vtrA] offset:17408\n\t"
    "v_mfma_f32_16x16x32_bf16 v[104:107], a[20:23], a[52:55], v[104:107]\n\t"
    "ds_read_b64_tr_b16 a[4:5], %[vtrA] offset:24576\n\t"
    "v_mfma_f32_16x16x32_bf16 v[108:111], a[16:19], a[56:59], v[108:111]\n\t"
    "ds_read_b64_tr_b16 a[6:7], %[vtrA] offset:25600\n\t"
    "v_mfma_f32_16x16x32_bf16 v[108:111], a[20:23], a[60:63], v[108:111]\n\t"
    "s_waitcnt lgkmcnt(4)\n\t"
    "v_mfma_f32_16x16x32_bf16 v[112:115], a[24:27], a[32:35], v[112:115]\n\t"
    "s_cmp_lg_u32 s45, 4\n\t"
    "s_cbranch_scc1 L_g1_x7_%=\n\t"
    "buffer_load_dwordx4 a[124:127], %[vtok], %[stok], s57 offen offset:3072 sc1\n\t"
    "L_g1_x7_%=:\n\t"
    "v_mfma_f32_16x16x32_bf16 v[112:115], a[28:31], a[36:39], v[112:115]\n\t"
    "v_xor_b32 %[vt], 32, %[vtrA]\n\t"
    "v_mfma_f32_16x16x32_bf16 v[116:119], a[24:27], a[40:43], v[116:119]\n\t"
    "buffer_load_dwordx4 a[144:147], %[vtok], %[stok], s50 offen offset:2048 sc1\n\t"
    "v_mfma_f32_16x16x32_bf16 v[116:119], a[28:31], a[44:47], v[116:119]\n\t"
    "ds_read_b64_tr_b16 a[8:9], %[vt] offset:16384\n\t"
    "v_mfma_f32_16x16x32_bf16 v[120:123], a[24:27], a[48:51], v[120:123]\n\t"
    "ds_read_b64_tr_b16 a[10:11], %[vt] offset:17408\n\t"
    "v_mfma_f32_16x16x32_bf16 v[120:123], a[28:31], a[52:55], v[120:123]\n\t"
    "ds_read_b64_tr_b16 a[12:13], %[vt] offset:24576\n\t"
    "v_mfma_f32_16x16x32_bf16 v[124:127], a[24:27], a[56:59], v[124:127]\n\t"
    "ds_read_b64_tr_b16 a[14:15], %[vt] offset:25600\n\t"
    "v_mfma_f32_16x16x32_bf16 v[124:127], a[28:31], a[60:63], v[124:127]\n\t"
    "s_waitcnt lgkmcnt(4)\n\t"
    "v_mfma_f32_16x16x32_bf16 v[128:131], a[0:3], a[32:35], v[128:131]\n\t"
    "v_xor_b32 %[vt], 64, %[vtrA]\n\t"
    "v_mfma_f32_16x16x32_bf16 v[128:131], a[4:7], a[36:39], v[128:131]\n\t"
    "buffer_load_dwordx4 %[vdma1], %[sw], s43 offen offset:1024 nt lds\n\t"
    "v_mfma_f32_16x16x32_bf16 v[132:135], a[0:3], a[40:43], v[132:135]\n\t"
    "ds_read_b64_tr_b16 a[16:17], %[vt] offset:16384\n\t"
    "v_mfma_f32_16x16x32_bf16 v[132:135], a[4:7], a[44:47], v[132:135]\n\t"
    "v_mfma_f32_16x16x32_bf16 v[136:139], a[0:3], a[48:51], v[136:139]\n\t"
    "ds_read_b64_tr_b16 a[18:19], %[vt] offset:17408\n\t"
    "v_mfma_f32_16x16x32_bf16 v[136:139], a[4:7], a[52:55], v[136:139]\n\t"
    "ds_read_b64_tr_b16 a[20:21], %[vt] offset:24576\n\t"
    "v_mfma_f32_16x16x32_bf16 v[140:143], a[0:3], a[56:59], v[140:143]\n\t"
    "ds_read_b64_tr_b16 a[22:23], %[vt] offset:25600\n\t"
    "v_mfma_f32_16x16x32_bf16 v[140:143], a[4:7], a[60:63], v[140:143]\n\t"
    "s_waitcnt lgkmcnt(4)\n\t"
    "v_mfma_f32_16x16x32_bf16 v[144:147], a[8:11], a[32:35], v[144:147]\n\t"
    "v_xor_b32 %[vt], 96, %[vtrA]\n\t"
    "v_mfma_f32_16x16x32_bf16 v[144:147], a[12:15], a[36:39], v[144:147]\n\t"
    "buffer_load_dwordx4 a[148:151], %[vtok], %[stok], s51 offen offset:2048 sc1\n\t"
    "v_mfma_f32_16x16x32_bf16 v[148:151], a[8:11], a[40:43], v[148:151]\n\t"
    "ds_read_b64_tr_b16 a[24:25], %[vt] offset:16384\n\t"
    "v_mfma_f32_16x16x32_bf16 v[148:151], a[12:15], a[44:47], v[148:151]\n\t"
    "v_mfma_f32_16x16x32_bf16 v[152:155], a[8:11], a[48:51], v[152:155]\n\t"
    "ds_read_b64_tr_b16 a[26:27], %[vt] offset:17408\n\t"
    "v_mfma_f32_16x16x32_bf16 v[152:155], a[12:15], a[52:55], v[152:155]\n\t"
    "ds_read_b64_tr_b16 a[28:29], %[vt] offset:24576\n\t"
    "v_mfma_f32_16x16x32_bf16 v[156:159], a[8:11], a[56:59], v[156:159]\n\t"
    "ds_read_b64_tr_b16 a[30:31], %[vt] offset:25600\n\t"
    "v_mfma_f32_16x16x32_bf16 v[156:159], a[12:15], a[60:63], v[156:159]\n\t"
    "s_waitcnt lgkmcnt(4)\n\t"
    "v_mfma_f32_16x16x32_bf16 v[160:163], a[16:19], a[32:35], v[160:163]\n\t"
    "v_xor_b32 %[vt], 128, %[vtrA]\n\t"
    "v_mfma_f32_16x16x32_bf16 v[160:163], a[20:23], a[36:39], v[160:163]\n\t"
    "buffer_load_dwordx4 %[vdma2], %[sw], s43 offen offset:2048 nt lds\n\t"
    "v_mfma_f32_16x16x32_bf16 v[164:167], a[16:19], a[40:43], v[164:167]\n\t"
    "ds_read_b64_tr_b16 a[0:1], %[vt] offset:16384\n\t"
    "v_mfma_f32_16x16x32_bf16 v[164:167], a[20:23], a[44:47], v[164:167]\n\t"
    "v_mfma_f32_16x16x32_bf16 v[168:171], a[16:19], a[48:51], v[168:171]\n\t"
    "ds_read_b64_tr_b16 a[2:3], %[vt] offset:17408\n\t"
    "v_mfma_f32_16x16x32_bf16 v[168:171], a[20:23], a[52:55], v[168:171]\n\t"
    "ds_read_b64_tr_b16 a[4:5], %[vt] offset:24576\n\t"
    "v_mfma_f32_16x16x32_bf16 v[172:175], a[16:19], a[56:59], v[172:175]\n\t"
    "ds_read_b64_tr_b16 a[6:7], %[vt] offset:25600\n\t"
    "v_mfma_f32_16x16x32_bf16 v[172:175], a[20:23], a[60:63], v[172:175]\n\t"
    "s_waitcnt lgkmcnt(4)\n\t"
    "v_mfma_f32_16x16x32_bf16 v[176:179], a[24:27], a[32:35], v[176:179]\n\t"
    "v_xor_b32 %[vt], 160, %[vtrA]\n\t"
    "v_mfma_f32_16x16x32_bf16 v[176:179], a[28:31], a[36:39], v[176:179]\n\t"
    "buffer_load_dwordx4 a[152:155], %[vtok], %[stok], s50 offen offset:3072 sc1\n\t"
    "v_mfma_f32_16x16x32_bf16 v[180:183], a[24:27], a[40:43], v[180:183]\n\t"
    "ds_read_b64_tr_b16 a[8:9], %[vt] offset:16384\n\t"
    "v_mfma_f32_16x16x32_bf16 v[180:183], a[28:31], a[44:47], v[180:183]\n\t"
    "v_mfma_f32_16x16x32_bf16 v[184:187], a[24:27], a[48:51], v[184:187]\n\t"
    "ds_read_b64_tr_b16 a[10:11], %[vt] offset:17408\n\t"
    "v_mfma_f32_16x16x32_bf16 v[184:187], a[28:31], a[52:55], v[184:187]\n\t"
    "ds_read_b64_tr_b16 a[12:13], %[vt] offset:24576\n\t"
    "v_mfma_f32_16x16x32_bf16 v[188:191], a[24:27], a[56:59], v[188:191]\n\t"
    "ds_read_b64_tr_b16 a[14:15], %[vt] offset:25600\n\t"
    "v_mfma_f32_16x16x32_bf16 v[188:191], a[28:31], a[60:63], v[188:191]\n\t"
    "s_waitcnt lgkmcnt(4)\n\t"
    "v_mfma_f32_16x16x32_bf16 v[192:195], a[0:3], a[32:35], v[192:195]\n\t"
    "v_xor_b32 %[vt], 192, %[vtrA]\n\t"
    "buffer_load_dwordx4 %[vdma3], %[sw], s43 offen offset:3072 nt lds\n\t"
    "s_add_u32 s42, s42, 458752\n\t"
    "v_mfma_f32_16x16x32_bf16 v[192:195], a[4:7], a[36:39], v[192:195]\n\t"
    "ds_read_b64_tr_b16 a[16:17], %[vt] offset:16384\n\t"
    "v_mfma_f32_16x16x32_bf16 v[196:199], a[0:3], a[40:43], v[196:199]\n\t"
    "ds_read_b64_tr_b16 a[18:19], %[vt] offset:17408\n\t"
    "ds_read_b64_tr_b16 a[20:21], %[vt] offset:24576\n\t"
    "v_mfma_f32_16x16x32_bf16 v[196:199], a[4:7], a[44:47], v[196:199]\n\t"
    "ds_read_b64_tr_b16 a[22:23], %[vt] offset:25600\n\t"
    "v_mfma_f32_16x16x32_bf16 v[200:203], a[0:3], a[48:51], v[200:203]\n\t"
    "v_xor_b32 %[vt], 224, %[vtrA]\n\t"
    "v_mfma_f32_16x16x32_bf16 v[200:203], a[4:7], a[52:55], v[200:203]\n\t"
    "ds_read_b64_tr_b16 a[24:25], %[vt] offset:16384\n\t"
    "ds_read_b64_tr_b16 a[26:27], %[vt] offset:17408\n\t"
    "v_mfma_f32_16x16x32_bf16 v[204:207], a[0:3], a[56:59], v[204:207]\n\t"
    "ds_read_b64_tr_b16 a[28:29], %[vt] offset:24576\n\t"
    "v_mfma_f32_16x16x32_bf16 v[204:207], a[4:7], a[60:63], v[204:207]\n\t"
    "ds_read_b64_tr_b16 a[30:31], %[vt] offset:25600\n\t"
    "s_waitcnt lgkmcnt(8)\n\t"
    "v_mfma_f32_16x16x32_bf16 v[208:211], a[8:11], a[32:35], v[208:211]\n\t"
    "buffer_load_dwordx4 a[156:159], %[vtok], %[stok], s51 offen offset:3072 sc1\n\t"
    "s_add_u32 s50, s50, 262144\n\t"
    "s_add_u32 s51, s51, 262144\n\t"
    "v_mfma_f32_16x16x32_bf16 v[208:211], a[12:15], a[36:39], v[208:211]\n\t"
    "v_mfma_f32_16x16x32_bf16 v[212:215], a[8:11], a[40:43], v[212:215]\n\t"
    "v_mfma_f32_16x16x32_bf16 v[212:215], a[12:15], a[44:47], v[212:215]\n\t"
    "v_mfma_f32_16x16x32_bf16 v[216:219], a[8:11], a[48:51], v[216:219]\n\t"
    "v_mfma_f32_16x16x32_bf16 v[216:219], a[12:15], a[52:55], v[216:219]\n\t"
    "v_mfma_f32_16x16x32_bf16 v[220:223], a[8:11], a[56:59], v[220:223]\n\t"
    "v_mfma_f32_16x16x32_bf16 v[220:223], a[12:15], a[60:63], v[220:223]\n\t"
    "s_cmp_lg_u32 s45, 4\n\t"
    "s_cbranch_scc1 L_g1_w0_%=\n\t"
    "s_waitcnt vmcnt(24)\n\t"
    "L_g1_w0_%=:\n\t"
    "s_waitcnt vmcnt(32) lgkmcnt(0)\n\t"
    "s_barrier\n\t"
    "v_mfma_f32_16x16x32_bf16 v[224:227], a[16:19], a[32:35], v[224:227]\n\t"
    "ds_read_b64_tr_b16 a[0:1], %[vtrA] offset:32768\n\t"
    "v_mfma_f32_16x16x32_bf16 v[224:227], a[20:23], a[36:39], v[224:227]\n\t"
    "ds_read_b64_tr_b16 a[2:3], %[vtrA] offset:33792\n\t"
    "v_mfma_f32_16x16x32_bf16 v[228:231], a[16:19], a[40:43], v[228:231]\n\t"
    "v_mfma_f32_16x16x32_bf16 v[228:231], a[20:23], a[44:47], v[228:231]\n\t"
    "ds_read_b64_tr_b16 a[4:5], %[vtrA] offset:40960\n\t"
    "v_mfma_f32_16x16x32_bf16 v[232:235], a[16:19], a[48:51], v[232:235]\n\t"
    "ds_read_b64_tr_b16 a[6:7], %[vtrA] offset:41984\n\t"
    "v_mfma_f32_16x16x32_bf16 v[232:235], a[20:23], a[52:55], v[232:235]\n\t"
    "v_mfma_f32_16x16x32_bf16 v[236:239], a[16:19], a[56:59], v[236:239]\n\t"
    "s_add_u32 m0, %[sm0], 0\n\t"
    "s_add_u32 s43, s42, 0\n\t"
    "buffer_load_dwordx4 %[vdma0], %[sw], s43 offen offset:0 nt lds\n\t"
    "v_mfma_f32_16x16x32_bf16 v[236:239], a[20:23], a[60:63], v[236:239]\n\t"
    "v_mfma_f32_16x16x32_bf16 a[160:163], a[24:27], a[32:35], a[160:163]\n\t"
    "v_xor_b32 %[vt], 32, %[vtrA]\n\t"
    "v_mfma_f32_16x16x32_bf16 a[160:163], a[28:31], a[36:39], a[160:163]\n\t"
    "ds_read_b64_tr_b16 a[8:9], %[vt] offset:32768\n\t"
    "v_mfma_f32_16x16x32_bf16 a[164:167], a[24:27], a[40:43], a[164:167]\n\t"
    "ds_read_b64_tr_b16 a[10:11], %[vt] offset:33792\n\t"
    "v_mfma_f32_16x16x32_bf16 a[164:167], a[28:31], a[44:47], a[164:167]\n\t"
    "v_mfma_f32_16x16x32_bf16 a[168:171], a[24:27], a[48:51], a[168:171]\n\t"
    "ds_read_b64_tr_b16 a[12:13], %[vt] offset:40960\n\t"
    "v_mfma_f32_16x16x32_bf16 a[168:171], a[28:31], a[52:55], a[168:171]\n\t"
    "ds_read_b64_tr_b16 a[14:15], %[vt] offset:41984\n\t"
    "v_mfma_f32_16x16x32_bf16 a[172:175], a[24:27], a[56:59], a[172:175]\n\t"
    "buffer_load_dwordx4 %[vdma1], %[sw], s43 offen offset:1024 nt lds\n\t"
    "v_mfma_f32_16x16x32_bf16 a[172:175], a[28:31], a[60:63], a[172:175]\n\t"
    "s_waitcnt lgkmcnt(4)\n\t"
    "v_mfma_f32_16x16x32_bf16 v[0:3], a[0:3], a[64:67], v[0:3]\n\t"
    "v_xor_b32 %[vt], 64, %[vtrA]\n\t"
    "v_mfma_f32_16x16x32_bf16 v[0:3], a[4:7], a[68:71], v[0:3]\n\t"
    "buffer_load_dwordx4 a[32:35], %[vtok], %[stok], s50 offen offset:0 sc1\n\t"
    "v_mfma_f32_16x16x32_bf16 v[4:7], a[0:3], a[72:75], v[4:7]\n\t"
    "ds_read_b64_tr_b16 a[16:17], %[vt] offset:32768\n\t"
    "v_mfma_f32_16x16x32_bf16 v[4:7], a[4:7], a[76:79], v[4:7]\n\t"
    "v_mfma_f32_16x16x32_bf16 v[8:11], a[0:3], a[80:83], v[8:11]\n\t"
    "ds_read_b64_tr_b16 a[18:19], %[vt] offset:33792\n\t"
    "v_mfma_f32_16x16x32_bf16 v[8:11], a[4:7], a[84:87], v[8:11]\n\t"
    "ds_read_b64_tr_b16 a[20:21], %[vt] offset:40960\n\t"
    "v_mfma_f32_16x16x32_bf16 v[12:15], a[0:3], a[88:91], v[12:15]\n\t"
    "ds_read_b64_tr_b16 a[22:23], %[vt] offset:41984\n\t"
    "v_mfma_f32_16x16x32_bf16 v[12:15], a[4:7], a[92:95], v[12:15]\n\t"
    "s_waitcnt lgkmcnt(4)\n\t"
    "v_mfma_f32_16x16x32_bf16 v[16:19], a[8:11], a[64:67], v[16:19]\n\t"
    "v_xor_b32 %[vt], 96, %[vtrA]\n\t"
    "v_mfma_f32_16x16x32_bf16 v[16:19], a[12:15], a[68:71], v[16:19]\n\t"
    "buffer_load_dwordx4 %[vdma2], %[sw], s43 offen offset:2048 nt lds\n\t"
    "v_mfma_f32_16x16x32_bf16 v[20:23], a[8:11], a[72:75], v[20:23]\n\t"
    "ds_read_b64_tr_b16 a[24:25], %[vt] offset:32768\n\t"
    "v_mfma_f32_16x16x32_bf16 v[20:23], a[12:15], a[76:79], v[20:23]\n\t"
    "v_mfma_f32_16x16x32_bf16 v[24:27], a[8:11], a[80:83], v[24:27]\n\t"
    "ds_read_b64_tr_b16 a[26:27], %[vt] offset:33792\n\t"
    "v_mfma_f32_16x16x32_bf16 v[24:27], a[12:15], a[84:87], v[24:27]\n\t"
    "ds_read_b64_tr_b16 a[28:29], %[vt] offset:40960\n\t"
    "v_mfma_f32_16x16x32_bf16 v[28:31], a[8:11], a[88:91], v[28:31]\n\t"
    "ds_read_b64_tr_b16 a[30:31], %[vt] offset:41984\n\t"
    "v_mfma_f32_16x16x32_bf16 v[28:31], a[12:15], a[92:95], v[28:31]\n\t"
    "s_waitcnt lgkmcnt(4)\n\t"
    "v_mfma_f32_16x16x32_bf16 v[32:35], a[16:19], a[64:67], v[32:35]\n\t"
    "v_xor_b32 %[vt], 128, %[vtrA]\n\t"
    "v_mfma_f32_16x16x32_bf16 v[32:35], a[20:23], a[68:71], v[32:35]\n\t"
    "buffer_load_dwordx4 a[36:39], %[vtok], %[stok], s51 offen offset:0 sc1\n\t"
    "v_mfma_f32_16x16x32_bf16 v[36:39], a[16:19], a[72:75], v[36:39]\n\t"
    "ds_read_b64_tr_b16 a[0:1], %[vt] offset:32768\n\t"
    "v_mfma_f32_16x16x32_bf16 v[36:39], a[20:23], a[76:79], v[36:39]\n\t"
    "v_mfma_f32_16x16x32_bf16 v[40:43], a[16:19], a[80:83], v[40:43]\n\t"
    "ds_read_b64_tr_b16 a[2:3], %[vt] offset:33792\n\t"
    "v_mfma_f32_16x16x32_bf16 v[40:43], a[20:23], a[84:87], v[40:43]\n\t"
    "ds_read_b64_tr_b16 a[4:5], %[vt] offset:40960\n\t"
    "v_mfma_f32_16x16x32_bf16 v[44:47], a[16:19], a[88:91], v[44:47]\n\t"
    "ds_read_b64_tr_b16 a[6:7], %[vt] offset:41984\n\t"
    "v_mfma_f32_16x16x32_bf16 v[44:47], a[20:23], a[92:95], v[44:47]\n\t"
    "s_waitcnt lgkmcnt(4)\n\t"
    "v_mfma_f32_16x16x32_bf16 v[48:51], a[24:27], a[64:67], v[48:51]\n\t"
    "v_xor_b32 %[vt], 160, %[vtrA]\n\t"
    "v_mfma_f32_16x16x32_bf16 v[48:51], a[28:31], a[68:71], v[48:51]\n\t"
    "buffer_load_dwordx4 %[vdma3], %[sw], s43 offen offset:3072 nt lds\n\t"
    "v_mfma_f32_16x16x32_bf16 v[52:55], a[24:27], a[72:75], v[52:55]\n\t"
    "ds_read_b64_tr_b16 a[8:9], %[vt] offset:32768\n\t"
    "v_mfma_f32_16x16x32_bf16 v[52:55], a[28:31], a[76:79], v[52:55]\n\t"
    "v_mfma_f32_16x16x32_bf16 v[56:59], a[24:27], a[80:83], v[56:59]\n\t"
    "ds_read_b64_tr_b16 a[10:11], %[vt] offset:33792\n\t"
    "v_mfma_f32_16x16x32_bf16 v[56:59], a[28:31], a[84:87], v[56:59]\n\t"
    "ds_read_b64_tr_b16 a[12:13], %[vt] offset:40960\n\t"
    "v_mfma_f32_16x16x32_bf16 v[60:63], a[24:27], a[88:91], v[60:63]\n\t"
    "ds_read_b64_tr_b16 a[14:15], %[vt] offset:41984\n\t"
    "v_mfma_f32_16x16x32_bf16 v[60:63], a[28:31], a[92:95], v[60:63]\n\t"
    "s_waitcnt lgkmcnt(4)\n\t"
    "v_mfma_f32_16x16x32_bf16 v[64:67], a[0:3], a[64:67], v[64:67]\n\t"
    "v_xor_b32 %[vt], 192, %[vtrA]\n\t"
    "v_mfma_f32_16x16x32_bf16 v[64:67], a[4:7], a[68:71], v[64:67]\n\t"
    "buffer_load_dwordx4 a[40:43], %[vtok], %[stok], s50 offen offset:1024 sc1\n\t"
    "v_mfma_f32_16x16x32_bf16 v[68:71], a[0:3], a[72:75], v[68:71]\n\t"
    "ds_read_b64_tr_b16 a[16:17], %[vt] offset:32768\n\t"
    "v_mfma_f32_16x16x32_bf16 v[68:71], a[4:7], a[76:79], v[68:71]\n\t"
    "v_mfma_f32_16x16x32_bf16 v[72:75], a[0:3], a[80:83], v[72:75]\n\t"
    "ds_read_b64_tr_b16 a[18:19], %[vt] offset:33792\n\t"
    "v_mfma_f32_16x16x32_bf16 v[72:75], a[4:7], a[84:87], v[72:75]\n\t"
    "ds_read_b64_tr_b16 a[20:21], %[vt] offset:40960\n\t"
    "v_mfma_f32_16x16x32_bf16 v[76:79], a[0:3], a[88:91], v[76:79]\n\t"
    "ds_read_b64_tr_b16 a[22:23], %[vt] offset:41984\n\t"
    "v_mfma_f32_16x16x32_bf16 v[76:79], a[4:7], a[92:95], v[76:79]\n\t"
    "s_waitcnt lgkmcnt(4)\n\t"
    "v_mfma_f32_16x16x32_bf16 v[80:83], a[8:11], a[64:67], v[80:83]\n\t"
    "v_xor_b32 %[vt], 224, %[vtrA]\n\t"
    "v_mfma_f32_16x16x32_bf16 v[80:83], a[12:15], a[68:71], v[80:83]\n\t"
    "s_add_u32 m0, %[sm0], 4096\n\t"
    "s_add_u32 s43, s42, 114688\n\t"
    "buffer_load_dwordx4 %[vdma0], %[sw], s43 offen offset:0 nt lds\n\t"
    "v_mfma_f32_16x16x32_bf16 v[84:87], a[8:11], a[72:75], v[84:87]\n\t"
    "ds_read_b64_tr_b16 a[24:25], %[vt] offset:32768\n\t"
    "v_mfma_f32_16x16x32_bf16 v[84:87], a[12:15], a[76:79], v[84:87]\n\t"
    "v_mfma_f32_16x16x32_bf16 v[88:91], a[8:11], a[80:83], v[88:91]\n\t"
    "ds_read_b64_tr_b16 a[26:27], %[vt] offset:33792\n\t"
    "v_mfma_f32_16x16x32_bf16 v[88:91], a[12:15], a[84:87], v[88:91]\n\t"
    "ds_read_b64_tr_b16 a[28:29], %[vt] offset:40960\n\t"
    "v_mfma_f32_16x16x32_bf16 v[92:95], a[8:11], a[88:91], v[92:95]\n\t"
    "ds_read_b64_tr_b16 a[30:31], %[vt] offset:41984\n\t"
    "v_mfma_f32_16x16x32_bf16 v[92:95], a[12:15], a[92:95], v[92:95]\n\t"
    "s_waitcnt lgkmcnt(4)\n\t"
    "v_mfma_f32_16x16x32_bf16 v[96:99], a[16:19], a[64:67], v[96:99]\n\t"
    "ds_read_b64_tr_b16 a[0:1], %[vtrA] offset:49152\n\t"
    "v_mfma_f32_16x16x32_bf16 v[96:99], a[20:23], a[68:71], v[96:99]\n\t"
    "buffer_load_dwordx4 a[44:47], %[vtok], %[stok], s51 offen offset:1024 sc1\n\t"
    "v_mfma_f32_16x16x32_bf16 v[100:103], a[16:19], a[72:75], v[100:103]\n\t"
    "v_mfma_f32_16x16x32_bf16 v[100:103], a[20:23], a[76:79], v[100:103]\n\t"
    "ds_read_b64_tr_b16 a[2:3], %[vtrA] offset:50176\n\t"
    "v_mfma_f32_16x16x32_bf16 v[104:107], a[16:19], a[80:83], v[104:107]\n\t"
    "ds_read_b64_tr_b16 a[4:5], %[vtrA] offset:57344\n\t"
    "v_mfma_f32_16x16x32_bf16 v[104:107], a[20:23], a[84:87], v[104:107]\n\t"
    "v_mfma_f32_16x16x32_bf16 v[108:111], a[16:19], a[88:91], v[108:111]\n\t"
    "ds_read_b64_tr_b16 a[6:7], %[vtrA] offset:58368\n\t"
    "v_mfma_f32_16x16x32_bf16 v[108:111], a[20:23], a[92:95], v[108:111]\n\t"
    "s_waitcnt lgkmcnt(4)\n\t"
    "v_mfma_f32_16x16x32_bf16 v[112:115], a[24:27], a[64:67], v[112:115]\n\t"
    "v_xor_b32 %[vt], 32, %[vtrA]\n\t"
    "v_mfma_f32_16x16x32_bf16 v[112:115], a[28:31], a[68:71], v[112:115]\n\t"
    "buffer_load_dwordx4 a[48:51], %[vtok], %[stok], s50 offen offset:2048 sc1\n\t"
    "v_mfma_f32_16x16x32_bf16 v[116:119], a[24:27], a[72:75], v[116:119]\n\t"
    "ds_read_b64_tr_b16 a[8:9], %[vt] offset:49152\n\t"
    "v_mfma_f32_16x16x32_bf16 v[116:119], a[28:31], a[76:79], v[116:119]\n\t"
    "v_mfma_f32_16x16x32_bf16 v[120:123], a[24:27], a[80:83], v[120:123]\n\t"
    "ds_read_b64_tr_b16 a[10:11], %[vt] offset:50176\n\t"
    "v_mfma_f32_16x16x32_bf16 v[120:123], a[28:31], a[84:87], v[120:123]\n\t"
    "ds_read_b64_tr_b16 a[12:13], %[vt] offset:57344\n\t"
    "v_mfma_f32_16x16x32_bf16 v[124:127], a[24:27], a[88:91], v[124:127]\n\t"
    "ds_read_b64_tr_b16 a[14:15], %[vt] offset:58368\n\t"
    "v_mfma_f32_16x16x32_bf16 v[124:127], a[28:31], a[92:95], v[124:127]\n\t"
    "s_waitcnt lgkmcnt(4)\n\t"
    "v_mfma_f32_16x16x32_bf16 v[128:131], a[0:3], a[64:67], v[128:131]\n\t"
    "v_xor_b32 %[vt], 64, %[vtrA]\n\t"
    "v_mfma_f32_16x16x32_bf16 v[128:131], a[4:7], a[68:71], v[128:131]\n\t"
    "buffer_load_dwordx4 %[vdma1], %[sw], s43 offen offset:1024 nt lds\n\t"
    "v_mfma_f32_16x16x32_bf16 v[132:135], a[0:3], a[72:75], v[132:135]\n\t"
    "ds_read_b64_tr_b16 a[16:17], %[vt] offset:49152\n\t"
    "v_mfma_f32_16x16x32_bf16 v[132:135], a[4:7], a[76:79], v[132:135]\n\t"
    "v_mfma_f32_16x16x32_bf16 v[136:139], a[0:3], a[80:83], v[136:139]\n\t"
    "ds_read_b64_tr_b16 a[18:19], %[vt] offset:50176\n\t"
    "v_mfma_f32_16x16x32_bf16 v[136:139], a[4:7], a[84:87], v[136:139]\n\t"
    "ds_read_b64_tr_b16 a[20:21], %[vt] offset:57344\n\t"
    "v_mfma_f32_16x16x32_bf16 v[140:143], a[0:3], a[88:91], v[140:143]\n\t"
    "ds_read_b64_tr_b16 a[22:23], %[vt] offset:58368\n\t"
    "v_mfma_f32_16x16x32_bf16 v[140:143], a[4:7], a[92:95], v[140:143]\n\t"
    "s_waitcnt lgkmcnt(4)\n\t"
    "v_mfma_f32_16x16x32_bf16 v[144:147], a[8:11], a[64:67], v[144:147]\n\t"
    "v_xor_b32 %[vt], 96, %[vtrA]\n\t"
    "v_mfma_f32_16x16x32_bf16 v[144:147], a[12:15], a[68:71], v[144:147]\n\t"
    "buffer_load_dwordx4 a[52:55], %[vtok], %[stok], s51 offen offset:2048 sc1\n\t"
    "v_mfma_f32_16x16x32_bf16 v[148:151], a[8:11], a[72:75], v[148:151]\n\t"
    "ds_read_b64_tr_b16 a[24:25], %[vt] offset:49152\n\t"
    "v_mfma_f32_16x16x32_bf16 v[148:151], a[12:15], a[76:79], v[148:151]\n\t"
    "v_mfma_f32_16x16x32_bf16 v[152:155], a[8:11], a[80:83], v[152:155]\n\t"
    "ds_read_b64_tr_b16 a[26:27], %[vt] offset:50176\n\t"
    "v_mfma_f32_16x16x32_bf16 v[152:155], a[12:15], a[84:87], v[152:155]\n\t"
    "ds_read_b64_tr_b16 a[28:29], %[vt] offset:57344\n\t"
    "v_mfma_f32_16x16x32_bf16 v[156:159], a[8:11], a[88:91], v[156:159]\n\t"
    "ds_read_b64_tr_b16 a[30:31], %[vt] offset:58368\n\t"
    "v_mfma_f32_16x16x32_bf16 v[156:159], a[12:15], a[92:95], v[156:159]\n\t"
    "s_waitcnt lgkmcnt(4)\n\t"
    "v_mfma_f32_16x16x32_bf16 v[160:163], a[16:19], a[64:67], v[160:163]\n\t"
    "v_xor_b32 %[vt], 128, %[vtrA]\n\t"
    "v_mfma_f32_16x16x32_bf16 v[160:163], a[20:23], a[68:71], v[160:163]\n\t"
    "buffer_load_dwordx4 %[vdma2], %[sw], s43 offen offset:2048 nt lds\n\t"
    "v_mfma_f32_16x16x32_bf16 v[164:167], a[16:19], a[72:75], v[164:167]\n\t"
    "ds_read_b64_tr_b16 a[0:1], %[vt] offset:49152\n\t"
    "v_mfma_f32_16x16x32_bf16 v[164:167], a[20:23], a[76:79], v[164:167]\n\t"
    "v_mfma_f32_16x16x32_bf16 v[168:171], a[16:19], a[80:83], v[168:171]\n\t"
    "ds_read_b64_tr_b16 a[2:3], %[vt] offset:50176\n\t"
    "v_mfma_f32_16x16x32_bf16 v[168:171], a[20:23], a[84:87], v[168:171]\n\t"
    "ds_read_b64_tr_b16 a[4:5], %[vt] offset:57344\n\t"
    "v_mfma_f32_16x16x32_bf16 v[172:175], a[16:19], a[88:91], v[172:175]\n\t"
    "ds_read_b64_tr_b16 a[6:7], %[vt] offset:58368\n\t"
    "v_mfma_f32_16x16x32_bf16 v[172:175], a[20:23], a[92:95], v[172:175]\n\t"
    "s_waitcnt lgkmcnt(4)\n\t"
    "v_mfma_f32_16x16x32_bf16 v[176:179], a[24:27], a[64:67], v[176:179]\n\t"
    "v_xor_b32 %[vt], 160, %[vtrA]\n\t"
    "v_mfma_f32_16x16x32_bf16 v[176:179], a[28:31], a[68:71], v[176:179]\n\t"
    "buffer_load_dwordx4 a[56:59], %[vtok], %[stok], s50 offen offset:3072 sc1\n\t"
    "v_mfma_f32_16x16x32_bf16 v[180:183], a[24:27], a[72:75], v[180:183]\n\t"
    "ds_read_b64_tr_b16 a[8:9], %[vt] offset:49152\n\t"
    "v_mfma_f32_16x16x32_bf16 v[180:183], a[28:31], a[76:79], v[180:183]\n\t"
    "v_mfma_f32_16x16x32_bf16 v[184:187], a[24:27], a[80:83], v[184:187]\n\t"
    "ds_read_b64_tr_b16 a[10:11], %[vt] offset:50176\n\t"
    "v_mfma_f32_16x16x32_bf16 v[184:187], a[28:31], a[84:87], v[184:187]\n\t"
    "ds_read_b64_tr_b16 a[12:13], %[vt] offset:57344\n\t"
    "v_mfma_f32_16x16x32_bf16 v[188:191], a[24:27], a[88:91], v[188:191]\n\t"
    "ds_read_b64_tr_b16 a[14:15], %[vt] offset:58368\n\t"
    "v_mfma_f32_16x16x32_bf16 v[188:191], a[28:31], a[92:95], v[188:191]\n\t"
    "s_waitcnt lgkmcnt(4)\n\t"
    "v_mfma_f32_16x16x32_bf16 v[192:195], a[0:3], a[64:67], v[192:195]\n\t"
    "v_xor_b32 %[vt], 192, %[vtrA]\n\t"
    "buffer_load_dwordx4 %[vdma3], %[sw], s43 offen offset:3072 nt lds\n\t"
    "s_add_u32 s42, s42, 458752\n\t"
    "v_mfma_f32_16x16x32_bf16 v[192:195], a[4:7], a[68:71], v[192:195]\n\t"
    "ds_read_b64_tr_b16 a[16:17], %[vt] offset:49152\n\t"
    "v_mfma_f32_16x16x32_bf16 v[196:199], a[0:3], a[72:75], v[196:199]\n\t"
    "ds_read_b64_tr_b16 a[18:19], %[vt] offset:50176\n\t"
    "ds_read_b64_tr_b16 a[20:21], %[vt] offset:57344\n\t"
    "v_mfma_f32_16x16x32_bf16 v[196:199], a[4:7], a[76:79], v[196:199]\n\t"
    "ds_read_b64_tr_b16 a[22:23], %[vt] offset:58368\n\t"
    "v_mfma_f32_16x16x32_bf16 v[200:203], a[0:3], a[80:83], v[200:203]\n\t"
    "v_xor_b32 %[vt], 224, %[vtrA]\n\t"
    "v_mfma_f32_16x16x32_bf16 v[200:203], a[4:7], a[84:87], v[200:203]\n\t"
    "ds_read_b64_tr_b16 a[24:25], %[vt] offset:49152\n\t"
    "ds_read_b64_tr_b16 a[26:27], %[vt] offset:50176\n\t"
    "v_mfma_f32_16x16x32_bf16 v[204:207], a[0:3], a[88:91], v[204:207]\n\t"
    "ds_read_b64_tr_b16 a[28:29], %[vt] offset:57344\n\t"
    "v_mfma_f32_16x16x32_bf16 v[204:207], a[4:7], a[92:95], v[204:207]\n\t"
    "ds_read_b64_tr_b16 a[30:31], %[vt] offset:58368\n\t"
    "s_waitcnt lgkmcnt(8)\n\t"
    "v_mfma_f32_16x16x32_bf16 v[208:211], a[8:11], a[64:67], v[208:211]\n\t"
    "buffer_load_dwordx4 a[60:63], %[vtok], %[stok], s51 offen offset:3072 sc1\n\t"
    "s_add_u32 s50, s50, 262144\n\t"
    "s_add_u32 s51, s51, 262144\n\t"
    "v_mfma_f32_16x16x32_bf16 v[208:211], a[12:15], a[68:71], v[208:211]\n\t"
    "v_mfma_f32_16x16x32_bf16 v[212:215], a[8:11], a[72:75], v[212:215]\n\t"
    "v_mfma_f32_16x16x32_bf16 v[212:215], a[12:15], a[76:79], v[212:215]\n\t"
    "v_mfma_f32_16x16x32_bf16 v[216:219], a[8:11], a[80:83], v[216:219]\n\t"
    "v_mfma_f32_16x16x32_bf16 v[216:219], a[12:15], a[84:87], v[216:219]\n\t"
    "v_mfma_f32_16x16x32_bf16 v[220:223], a[8:11], a[88:91], v[220:223]\n\t"
    "v_mfma_f32_16x16x32_bf16 v[220:223], a[12:15], a[92:95], v[220:223]\n\t"
    "s_cmp_lg_u32 s45, 4\n\t"
    "s_cbranch_scc1 L_g1_w1_%=\n\t"
    "s_waitcnt vmcnt(23)\n\t"
    "L_g1_w1_%=:\n\t"
    "s_waitcnt vmcnt(32) lgkmcnt(0)\n\t"
    "s_barrier\n\t"
    "v_mfma_f32_16x16x32_bf16 v[224:227], a[16:19], a[64:67], v[224:227]\n\t"
    "ds_read_b64_tr_b16 a[0:1], %[vtrB] offset:0\n\t"
    "v_mfma_f32_16x16x32_bf16 v[224:227], a[20:23], a[68:71], v[224:227]\n\t"
    "ds_read_b64_tr_b16 a[2:3], %[vtrB] offset:1024\n\t"
    "v_mfma_f32_16x16x32_bf16 v[228:231], a[16:19], a[72:75], v[228:231]\n\t"
    "v_mfma_f32_16x16x32_bf16 v[228:231], a[20:23], a[76:79], v[228:231]\n\t"
    "ds_read_b64_tr_b16 a[4:5], %[vtrB] offset:8192\n\t"
    "v_mfma_f32_16x16x32_bf16 v[232:235], a[16:19], a[80:83], v[232:235]\n\t"
    "ds_read_b64_tr_b16 a[6:7], %[vtrB] offset:9216\n\t"
    "v_mfma_f32_16x16x32_bf16 v[232:235], a[20:23], a[84:87], v[232:235]\n\t"
    "v_mfma_f32_16x16x32_bf16 v[236:239], a[16:19], a[88:91], v[236:239]\n\t"
    "s_add_u32 m0, %[sm0], 32768\n\t"
    "s_add_u32 s43, s42, 0\n\t"
    "buffer_load_dwordx4 %[vdma0], %[sw], s43 offen offset:0 nt lds\n\t"
    "v_mfma_f32_16x16x32_bf16 v[236:239], a[20:23], a[92:95], v[236:239]\n\t"
    "v_mfma_f32_16x16x32_bf16 a[160:163], a[24:27], a[64:67], a[160:163]\n\t"
    "v_xor_b32 %[vt], 32, %[vtrB]\n\t"
    "v_mfma_f32_16x16x32_bf16 a[160:163], a[28:31], a[68:71], a[160:163]\n\t"
    "ds_read_b64_tr_b16 a[8:9], %[vt] offset:0\n\t"
    "v_mfma_f32_16x16x32_bf16 a[164:167], a[24:27], a[72:75], a[164:167]\n\t"
    "ds_read_b64_tr_b16 a[10:11], %[vt] offset:1024\n\t"
    "v_mfma_f32_16x16x32_bf16 a[164:167], a[28:31], a[76:79], a[164:167]\n\t"
    "v_mfma_f32_16x16x32_bf16 a[168:171], a[24:27], a[80:83], a[168:171]\n\t"
    "ds_read_b64_tr_b16 a[12:13], %[vt] offset:8192\n\t"
    "v_mfma_f32_16x16x32_bf16 a[168:171], a[28:31], a[84:87], a[168:171]\n\t"
    "ds_read_b64_tr_b16 a[14:15], %[vt] offset:9216\n\t"
    "v_mfma_f32_16x16x32_bf16 a[172:175], a[24:27], a[88:91], a[172:175]\n\t"
    "buffer_load_dwordx4 %[vdma1], %[sw], s43 offen offset:1024 nt lds\n\t"
    "v_mfma_f32_16x16x32_bf16 a[172:175], a[28:31], a[92:95], a[172:175]\n\t"
    "s_waitcnt lgkmcnt(4)\n\t"
    "v_mfma_f32_16x16x32_bf16 v[0:3], a[0:3], a[96:99], v[0:3]\n\t"
    "v_xor_b32 %[vt], 64, %[vtrB]\n\t"
    "v_mfma_f32_16x16x32_bf16 v[0:3], a[4:7], a[100:103], v[0:3]\n\t"
    "buffer_load_dwordx4 a[64:67], %[vtok], %[stok], s50 offen offset:0 sc1\n\t"
    "v_mfma_f32_16x16x32_bf16 v[4:7], a[0:3], a[104:107], v[4:7]\n\t"
    "ds_read_b64_tr_b16 a[16:17], %[vt] offset:0\n\t"
    "v_mfma_f32_16x16x32_bf16 v[4:7], a[4:7], a[108:111], v[4:7]\n\t"
    "v_mfma_f32_16x16x32_bf16 v[8:11], a[0:3], a[112:115], v[8:11]\n\t"
    "ds_read_b64_tr_b16 a[18:19], %[vt] offset:1024\n\t"
    "v_mfma_f32_16x16x32_bf16 v[8:11], a[4:7], a[116:119], v[8:11]\n\t"
    "ds_read_b64_tr_b16 a[20:21], %[vt] offset:8192\n\t"
    "v_mfma_f32_16x16x32_bf16 v[12:15], a[0:3], a[120:123], v[12:15]\n\t"
    "ds_read_b64_tr_b16 a[22:23], %[vt] offset:9216\n\t"
    "v_mfma_f32_16x16x32_bf16 v[12:15], a[4:7], a[124:127], v[12:15]\n\t"
    "s_waitcnt lgkmcnt(4)\n\t"
    "v_mfma_f32_16x16x32_bf16 v[16:19], a[8:11], a[96:99], v[16:19]\n\t"
    "v_xor_b32 %[vt], 96, %[vtrB]\n\t"
    "v_mfma_f32_16x16x32_bf16 v[16:19], a[12:15], a[100:103], v[16:19]\n\t"
    "buffer_load_dwordx4 %[vdma2], %[sw], s43 offen offset:2048 nt lds\n\t"
    "v_mfma_f32_16x16x32_bf16 v[20:23], a[8:11], a[104:107], v[20:23]\n\t"
    "ds_read_b64_tr_b16 a[24:25], %[vt] offset:0\n\t"
    "v_mfma_f32_16x16x32_bf16 v[20:23], a[12:15], a[108:111], v[20:23]\n\t"
    "v_mfma_f32_16x16x32_bf16 v[24:27], a[8:11], a[112:115], v[24:27]\n\t"
    "ds_read_b64_tr_b16 a[26:27], %[vt] offset:1024\n\t"
    "v_mfma_f32_16x16x32_bf16 v[24:27], a[12:15], a[116:119], v[24:27]\n\t"
    "ds_read_b64_tr_b16 a[28:29], %[vt] offset:8192\n\t"
    "v_mfma_f32_16x16x32_bf16 v[28:31], a[8:11], a[120:123], v[28:31]\n\t"
    "ds_read_b64_tr_b16 a[30:31], %[vt] offset:9216\n\t"
    "v_mfma_f32_16x16x32_bf16 v[28:31], a[12:15], a[124:127], v[28:31]\n\t"
    "s_waitcnt lgkmcnt(4)\n\t"
    "v_mfma_f32_16x16x32_bf16 v[32:35], a[16:19], a[96:99], v[32:35]\n\t"
    "v_xor_b32 %[vt], 128, %[vtrB]\n\t"
    "v_mfma_f32_16x16x32_bf16 v[32:35], a[20:23], a[100:103], v[32:35]\n\t"
    "buffer_load_dwordx4 a[68:71], %[vtok], %[stok], s51 offen offset:0 sc1\n\t"
    "v_mfma_f32_16x16x32_bf16 v[36:39], a[16:19], a[104:107], v[36:39]\n\t"
    "ds_read_b64_tr_b16 a[0:1], %[vt] offset:0\n\t"
    "v_mfma_f32_16x16x32_bf16 v[36:39], a[20:23], a[108:111], v[36:39]\n\t"
    "v_mfma_f32_16x16x32_bf16 v[40:43], a[16:19], a[112:115], v[40:43]\n\t"
    "ds_read_b64_tr_b16 a[2:3], %[vt] offset:1024\n\t"
    "v_mfma_f32_16x16x32_bf16 v[40:43], a[20:23], a[116:119], v[40:43]\n\t"
    "ds_read_b64_tr_b16 a[4:5], %[vt] offset:8192\n\t"
    "v_mfma_f32_16x16x32_bf16 v[44:47], a[16:19], a[120:123], v[44:47]\n\t"
    "ds_read_b64_tr_b16 a[6:7], %[vt] offset:9216\n\t"
    "v_mfma_f32_16x16x32_bf16 v[44:47], a[20:23], a[124:127], v[44:47]\n\t"
    "s_waitcnt lgkmcnt(4)\n\t"
    "v_mfma_f32_16x16x32_bf16 v[48:51], a[24:27], a[96:99], v[48:51]\n\t"
    "v_xor_b32 %[vt], 160, %[vtrB]\n\t"
    "v_mfma_f32_16x16x32_bf16 v[48:51], a[28:31], a[100:103], v[48:51]\n\t"
    "buffer_load_dwordx4 %[vdma3], %[sw], s43 offen offset:3072 nt lds\n\t"
    "v_mfma_f32_16x16x32_bf16 v[52:55], a[24:27], a[104:107], v[52:55]\n\t"
    "ds_read_b64_tr_b16 a[8:9], %[vt] offset:0\n\t"
    "v_mfma_f32_16x16x32_bf16 v[52:55], a[28:31], a[108:111], v[52:55]\n\t"
    "v_mfma_f32_16x16x32_bf16 v[56:59], a[24:27], a[112:115], v[56:59]\n\t"
    "ds_read_b64_tr_b16 a[10:11], %[vt] offset:1024\n\t"
    "v_mfma_f32_16x16x32_bf16 v[56:59], a[28:31], a[116:119], v[56:59]\n\t"
    "ds_read_b64_tr_b16 a[12:13], %[vt] offset:8192\n\t"
    "v_mfma_f32_16x16x32_bf16 v[60:63], a[24:27], a[120:123], v[60:63]\n\t"
    "ds_read_b64_tr_b16 a[14:15], %[vt] offset:9216\n\t"
    "v_mfma_f32_16x16x32_bf16 v[60:63], a[28:31], a[124:127], v[60:63]\n\t"
    "s_waitcnt lgkmcnt(4)\n\t"
    "v_mfma_f32_16x16x32_bf16 v[64:67], a[0:3], a[96:99], v[64:67]\n\t"
    "v_xor_b32 %[vt], 192, %[vtrB]\n\t"
    "v_mfma_f32_16x16x32_bf16 v[64:67], a[4:7], a[100:103], v[64:67]\n\t"
    "buffer_load_dwordx4 a[72:75], %[vtok], %[stok], s50 offen offset:1024 sc1\n\t"
    "v_mfma_f32_16x16x32_bf16 v[68:71], a[0:3], a[104:107], v[68:71]\n\t"
    "ds_read_b64_tr_b16 a[16:17], %[vt] offset:0\n\t"
    "v_mfma_f32_16x16x32_bf16 v[68:71], a[4:7], a[108:111], v[68:71]\n\t"
    "v_mfma_f32_16x16x32_bf16 v[72:75], a[0:3], a[112:115], v[72:75]\n\t"
    "ds_read_b64_tr_b16 a[18:19], %[vt] offset:1024\n\t"
    "v_mfma_f32_16x16x32_bf16 v[72:75], a[4:7], a[116:119], v[72:75]\n\t"
    "ds_read_b64_tr_b16 a[20:21], %[vt] offset:8192\n\t"
    "v_mfma_f32_16x16x32_bf16 v[76:79], a[0:3], a[120:123], v[76:79]\n\t"
    "ds_read_b64_tr_b16 a[22:23], %[vt] offset:9216\n\t"
    "v_mfma_f32_16x16x32_bf16 v[76:79], a[4:7], a[124:127], v[76:79]\n\t"
    "s_waitcnt lgkmcnt(4)\n\t"
    "v_mfma_f32_16x16x32_bf16 v[80:83], a[8:11], a[96:99], v[80:83]\n\t"
    "v_xor_b32 %[vt], 224, %[vtrB]\n\t"
    "v_mfma_f32_16x16x32_bf16 v[80:83], a[12:15], a[100:103], v[80:83]\n\t"
    "s_add_u32 m0, %[sm0], 36864\n\t"
    "s_add_u32 s43, s42, 114688\n\t"
    "buffer_load_dwordx4 %[vdma0], %[sw], s43 offen offset:0 nt lds\n\t"
    "v_mfma_f32_16x16x32_bf16 v[84:87], a[8:11], a[104:107], v[84:87]\n\t"
    "ds_read_b64_tr_b16 a[24:25], %[vt] offset:0\n\t"
    "v_mfma_f32_16x16x32_bf16 v[84:87], a[12:15], a[108:111], v[84:87]\n\t"
    "v_mfma_f32_16x16x32_bf16 v[88:91], a[8:11], a[112:115], v[88:91]\n\t"
    "ds_read_b64_tr_b16 a[26:27], %[vt] offset:1024\n\t"
    "v_mfma_f32_16x16x32_bf16 v[88:91], a[12:15], a[116:119], v[88:91]\n\t"
    "ds_read_b64_tr_b16 a[28:29], %[vt] offset:8192\n\t"
    "v_mfma_f32_16x16x32_bf16 v[92:95], a[8:11], a[120:123], v[92:95]\n\t"
    "ds_read_b64_tr_b16 a[30:31], %[vt] offset:9216\n\t"
    "v_mfma_f32_16x16x32_bf16 v[92:95], a[12:15], a[124:127], v[92:95]\n\t"
    "s_waitcnt lgkmcnt(4)\n\t"
    "v_mfma_f32_16x16x32_bf16 v[96:99], a[16:19], a[96:99], v[96:99]\n\t"
    "ds_read_b64_tr_b16 a[0:1], %[vtrB] offset:16384\n\t"
    "v_mfma_f32_16x16x32_bf16 v[96:99], a[20:23], a[100:103], v[96:99]\n\t"
    "buffer_load_dwordx4 a[76:79], %[vtok], %[stok], s51 offen offset:1024 sc1\n\t"
    "v_mfma_f32_16x16x32_bf16 v[100:103], a[16:19], a[104:107], v[100:103]\n\t"
    "v_mfma_f32_16x16x32_bf16 v[100:103], a[20:23], a[108:111], v[100:103]\n\t"
    "ds_read_b64_tr_b16 a[2:3], %[vtrB] offset:17408\n\t"
    "v_mfma_f32_16x16x32_bf16 v[104:107], a[16:19], a[112:115], v[104:107]\n\t"
    "ds_read_b64_tr_b16 a[4:5], %[vtrB] offset:24576\n\t"
    "v_mfma_f32_16x16x32_bf16 v[104:107], a[20:23], a[116:119], v[104:107]\n\t"
    "v_mfma_f32_16x16x32_bf16 v[108:111], a[16:19], a[120:123], v[108:111]\n\t"
    "ds_read_b64_tr_b16 a[6:7], %[vtrB] offset:25600\n\t"
    "v_mfma_f32_16x16x32_bf16 v[108:111], a[20:23], a[124:127], v[108:111]\n\t"
    "s_waitcnt lgkmcnt(4)\n\t"
    "v_mfma_f32_16x16x32_bf16 v[112:115], a[24:27], a[96:99], v[112:115]\n\t"
    "v_xor_b32 %[vt], 32, %[vtrB]\n\t"
    "v_mfma_f32_16x16x32_bf16 v[112:115], a[28:31], a[100:103], v[112:115]\n\t"
    "buffer_load_dwordx4 a[80:83], %[vtok], %[stok], s50 offen offset:2048 sc1\n\t"
    "v_mfma_f32_16x16x32_bf16 v[116:119], a[24:27], a[104:107], v[116:119]\n\t"
    "ds_read_b64_tr_b16 a[8:9], %[vt] offset:16384\n\t"
    "v_mfma_f32_16x16x32_bf16 v[116:119], a[28:31], a[108:111], v[116:119]\n\t"
    "v_mfma_f32_16x16x32_bf16 v[120:123], a[24:27], a[112:115], v[120:123]\n\t"
    "ds_read_b64_tr_b16 a[10:11], %[vt] offset:17408\n\t"
    "v_mfma_f32_16x16x32_bf16 v[120:123], a[28:31], a[116:119], v[120:123]\n\t"
    "ds_read_b64_tr_b16 a[12:13], %[vt] offset:24576\n\t"
    "v_mfma_f32_16x16x32_bf16 v[124:127], a[24:27], a[120:123], v[124:127]\n\t"
    "ds_read_b64_tr_b16 a[14:15], %[vt] offset:25600\n\t"
    "v_mfma_f32_16x16x32_bf16 v[124:127], a[28:31], a[124:127], v[124:127]\n\t"
    "s_waitcnt lgkmcnt(4)\n\t"
    "v_mfma_f32_16x16x32_bf16 v[128:131], a[0:3], a[96:99], v[128:131]\n\t"
    "v_xor_b32 %[vt], 64, %[vtrB]\n\t"
    "v_mfma_f32_16x16x32_bf16 v[128:131], a[4:7], a[100:103], v[128:131]\n\t"
    "buffer_load_dwordx4 %[vdma1], %[sw], s43 offen offset:1024 nt lds\n\t"
    "v_mfma_f32_16x16x32_bf16 v[132:135], a[0:3], a[104:107], v[132:135]\n\t"
    "ds_read_b64_tr_b16 a[16:17], %[vt] offset:16384\n\t"
    "v_mfma_f32_16x16x32_bf16 v[132:135], a[4:7], a[108:111], v[132:135]\n\t"
    "v_mfma_f32_16x16x32_bf16 v[136:139], a[0:3], a[112:115], v[136:139]\n\t"
    "ds_read_b64_tr_b16 a[18:19], %[vt] offset:17408\n\t"
    "v_mfma_f32_16x16x32_bf16 v[136:139], a[4:7], a[116:119], v[136:139]\n\t"
    "ds_read_b64_tr_b16 a[20:21], %[vt] offset:24576\n\t"
    "v_mfma_f32_16x16x32_bf16 v[140:143], a[0:3], a[120:123], v[140:143]\n\t"
    "ds_read_b64_tr_b16 a[22:23], %[vt] offset:25600\n\t"
    "v_mfma_f32_16x16x32_bf16 v[140:143], a[4:7], a[124:127], v[140:143]\n\t"
    "s_waitcnt lgkmcnt(4)\n\t"
    "v_mfma_f32_16x16x32_bf16 v[144:147], a[8:11], a[96:99], v[144:147]\n\t"
    "v_xor_b32 %[vt], 96, %[vtrB]\n\t"
    "v_mfma_f32_16x16x32_bf16 v[144:147], a[12:15], a[100:103], v[144:147]\n\t"
    "buffer_load_dwordx4 a[84:87], %[vtok], %[stok], s51 offen offset:2048 sc1\n\t"
    "v_mfma_f32_16x16x32_bf16 v[148:151], a[8:11], a[104:107], v[148:151]\n\t"
    "ds_read_b64_tr_b16 a[24:25], %[vt] offset:16384\n\t"
    "v_mfma_f32_16x16x32_bf16 v[148:151], a[12:15], a[108:111], v[148:151]\n\t"
    "v_mfma_f32_16x16x32_bf16 v[152:155], a[8:11], a[112:115], v[152:155]\n\t"
    "ds_read_b64_tr_b16 a[26:27], %[vt] offset:17408\n\t"
    "v_mfma_f32_16x16x32_bf16 v[152:155], a[12:15], a[116:119], v[152:155]\n\t"
    "ds_read_b64_tr_b16 a[28:29], %[vt] offset:24576\n\t"
    "v_mfma_f32_16x16x32_bf16 v[156:159], a[8:11], a[120:123], v[156:159]\n\t"
    "ds_read_b64_tr_b16 a[30:31], %[vt] offset:25600\n\t"
    "v_mfma_f32_16x16x32_bf16 v[156:159], a[12:15], a[124:127], v[156:159]\n\t"
    "s_waitcnt lgkmcnt(4)\n\t"
    "v_mfma_f32_16x16x32_bf16 v[160:163], a[16:19], a[96:99], v[160:163]\n\t"
    "v_xor_b32 %[vt], 128, %[vtrB]\n\t"
    "v_mfma_f32_16x16x32_bf16 v[160:163], a[20:23], a[100:103], v[160:163]\n\t"
    "buffer_load_dwordx4 %[vdma2], %[sw], s43 offen offset:2048 nt lds\n\t"
    "v_mfma_f32_16x16x32_bf16 v[164:167], a[16:19], a[104:107], v[164:167]\n\t"
    "ds_read_b64_tr_b16 a[0:1], %[vt] offset:16384\n\t"
    "v_mfma_f32_16x16x32_bf16 v[164:167], a[20:23], a[108:111], v[164:167]\n\t"
    "v_mfma_f32_16x16x32_bf16 v[168:171], a[16:19], a[112:115], v[168:171]\n\t"
    "ds_read_b64_tr_b16 a[2:3], %[vt] offset:17408\n\t"
    "v_mfma_f32_16x16x32_bf16 v[168:171], a[20:23], a[116:119], v[168:171]\n\t"
    "ds_read_b64_tr_b16 a[4:5], %[vt] offset:24576\n\t"
    "v_mfma_f32_16x16x32_bf16 v[172:175], a[16:19], a[120:123], v[172:175]\n\t"
    "ds_read_b64_tr_b16 a[6:7], %[vt] offset:25600\n\t"
    "v_mfma_f32_16x16x32_bf16 v[172:175], a[20:23], a[124:127], v[172:175]\n\t"
    "s_waitcnt lgkmcnt(4)\n\t"
    "v_mfma_f32_16x16x32_bf16 v[176:179], a[24:27], a[96:99], v[176:179]\n\t"
    "v_xor_b32 %[vt], 160, %[vtrB]\n\t"
    "v_mfma_f32_16x16x32_bf16 v[176:179], a[28:31], a[100:103], v[176:179]\n\t"
    "buffer_load_dwordx4 a[88:91], %[vtok], %[stok], s50 offen offset:3072 sc1\n\t"
    "v_mfma_f32_16x16x32_bf16 v[180:183], a[24:27], a[104:107], v[180:183]\n\t"
    "ds_read_b64_tr_b16 a[8:9], %[vt] offset:16384\n\t"
    "v_mfma_f32_16x16x32_bf16 v[180:183], a[28:31], a[108:111], v[180:183]\n\t"
    "v_mfma_f32_16x16x32_bf16 v[184:187], a[24:27], a[112:115], v[184:187]\n\t"
    "ds_read_b64_tr_b16 a[10:11], %[vt] offset:17408\n\t"
    "v_mfma_f32_16x16x32_bf16 v[184:187], a[28:31], a[116:119], v[184:187]\n\t"
    "ds_read_b64_tr_b16 a[12:13], %[vt] offset:24576\n\t"
    "v_mfma_f32_16x16x32_bf16 v[188:191], a[24:27], a[120:123], v[188:191]\n\t"
    "ds_read_b64_tr_b16 a[14:15], %[vt] offset:25600\n\t"
    "v_mfma_f32_16x16x32_bf16 v[188:191], a[28:31], a[124:127], v[188:191]\n\t"
    "s_waitcnt lgkmcnt(4)\n\t"
    "v_mfma_f32_16x16x32_bf16 v[192:195], a[0:3], a[96:99], v[192:195]\n\t"
    "v_xor_b32 %[vt], 192, %[vtrB]\n\t"
    "buffer_load_dwordx4 %[vdma3], %[sw], s43 offen offset:3072 nt lds\n\t"
    "s_add_u32 s42, s42, 458752\n\t"
    "v_mfma_f32_16x16x32_bf16 v[192:195], a[4:7], a[100:103], v[192:195]\n\t"
    "ds_read_b64_tr_b16 a[16:17], %[vt] offset:16384\n\t"
    "v_mfma_f32_16x16x32_bf16 v[196:199], a[0:3], a[104:107], v[196:199]\n\t"
    "ds_read_b64_tr_b16 a[18:19], %[vt] offset:17408\n\t"
    "ds_read_b64_tr_b16 a[20:21], %[vt] offset:24576\n\t"
    "v_mfma_f32_16x16x32_bf16 v[196:199], a[4:7], a[108:111], v[196:199]\n\t"
    "ds_read_b64_tr_b16 a[22:23], %[vt] offset:25600\n\t"
    "v_mfma_f32_16x16x32_bf16 v[200:203], a[0:3], a[112:115], v[200:203]\n\t"
    "v_xor_b32 %[vt], 224, %[vtrB]\n\t"
    "v_mfma_f32_16x16x32_bf16 v[200:203], a[4:7], a[116:119], v[200:203]\n\t"
    "ds_read_b64_tr_b16 a[24:25], %[vt] offset:16384\n\t"
    "ds_read_b64_tr_b16 a[26:27], %[vt] offset:17408\n\t"
    "v_mfma_f32_16x16x32_bf16 v[204:207], a[0:3], a[120:123], v[204:207]\n\t"
    "ds_read_b64_tr_b16 a[28:29], %[vt] offset:24576\n\t"
    "v_mfma_f32_16x16x32_bf16 v[204:207], a[4:7], a[124:127], v[204:207]\n\t"
    "ds_read_b64_tr_b16 a[30:31], %[vt] offset:25600\n\t"
    "s_waitcnt lgkmcnt(8)\n\t"
    "v_mfma_f32_16x16x32_bf16 v[208:211], a[8:11], a[96:99], v[208:211]\n\t"
    "buffer_load_dwordx4 a[92:95], %[vtok], %[stok], s51 offen offset:3072 sc1\n\t"
    "s_add_u32 s50, s50, 262144\n\t"
    "s_add_u32 s51, s51, 262144\n\t"
    "v_mfma_f32_16x16x32_bf16 v[208:211], a[12:15], a[100:103], v[208:211]\n\t"
    "v_mfma_f32_16x16x32_bf16 v[212:215], a[8:11], a[104:107], v[212:215]\n\t"
    "v_mfma_f32_16x16x32_bf16 v[212:215], a[12:15], a[108:111], v[212:215]\n\t"
    "v_mfma_f32_16x16x32_bf16 v[216:219], a[8:11], a[112:115], v[216:219]\n\t"
    "v_mfma_f32_16x16x32_bf16 v[216:219], a[12:15], a[116:119], v[216:219]\n\t"
    "v_mfma_f32_16x16x32_bf16 v[220:223], a[8:11], a[120:123], v[220:223]\n\t"
    "v_mfma_f32_16x16x32_bf16 v[220:223], a[12:15], a[124:127], v[220:223]\n\t"
    "s_waitcnt vmcnt(32) lgkmcnt(0)\n\t"
    "s_barrier\n\t"
    "v_mfma_f32_16x16x32_bf16 v[224:227], a[16:19], a[96:99], v[224:227]\n\t"
    "ds_read_b64_tr_b16 a[0:1], %[vtrB] offset:32768\n\t"
    "v_mfma_f32_16x16x32_bf16 v[224:227], a[20:23], a[100:103], v[224:227]\n\t"
    "ds_read_b64_tr_b16 a[2:3], %[vtrB] offset:33792\n\t"
    "v_mfma_f32_16x16x32_bf16 v[228:231], a[16:19], a[104:107], v[228:231]\n\t"
    "v_mfma_f32_16x16x32_bf16 v[228:231], a[20:23], a[108:111], v[228:231]\n\t"
    "ds_read_b64_tr_b16 a[4:5], %[vtrB] offset:40960\n\t"
    "v_mfma_f32_16x16x32_bf16 v[232:235], a[16:19], a[112:115], v[232:235]\n\t"
    "ds_read_b64_tr_b16 a[6:7], %[vtrB] offset:41984\n\t"
    "v_mfma_f32_16x16x32_bf16 v[232:235], a[20:23], a[116:119], v[232:235]\n\t"
    "v_mfma_f32_16x16x32_bf16 v[236:239], a[16:19], a[120:123], v[236:239]\n\t"
    "s_add_u32 m0, %[sm0], 65536\n\t"
    "s_add_u32 s43, s42, 0\n\t"
    "buffer_load_dwordx4 %[vdma0], %[sw], s43 offen offset:0 nt lds\n\t"
    "v_mfma_f32_16x16x32_bf16 v[236:239], a[20:23], a[124:127], v[236:239]\n\t"
    "v_mfma_f32_16x16x32_bf16 a[160:163], a[24:27], a[96:99], a[160:163]\n\t"
    "v_xor_b32 %[vt], 32, %[vtrB]\n\t"
    "v_mfma_f32_16x16x32_bf16 a[160:163], a[28:31], a[100:103], a[160:163]\n\t"
    "ds_read_b64_tr_b16 a[8:9], %[vt] offset:32768\n\t"
    "v_mfma_f32_16x16x32_bf16 a[164:167], a[24:27], a[104:107], a[164:167]\n\t"
    "ds_read_b64_tr_b16 a[10:11], %[vt] offset:33792\n\t"
    "v_mfma_f32_16x16x32_bf16 a[164:167], a[28:31], a[108:111], a[164:167]\n\t"
    "v_mfma_f32_16x16x32_bf16 a[168:171], a[24:27], a[112:115], a[168:171]\n\t"
    "ds_read_b64_tr_b16 a[12:13], %[vt] offset:40960\n\t"
    "v_mfma_f32_16x16x32_bf16 a[168:171], a[28:31], a[116:119], a[168:171]\n\t"
    "ds_read_b64_tr_b16 a[14:15], %[vt] offset:41984\n\t"
    "v_mfma_f32_16x16x32_bf16 a[172:175], a[24:27], a[120:123], a[172:175]\n\t"
    "buffer_load_dwordx4 %[vdma1], %[sw], s43 offen offset:1024 nt lds\n\t"
    "v_mfma_f32_16x16x32_bf16 a[172:175], a[28:31], a[124:127], a[172:175]\n\t"
    "s_waitcnt lgkmcnt(4)\n\t"
    "v_mfma_f32_16x16x32_bf16 v[0:3], a[0:3], a[128:131], v[0:3]\n\t"
    "v_xor_b32 %[vt], 64, %[vtrB]\n\t"
    "v_mfma_f32_16x16x32_bf16 v[0:3], a[4:7], a[132:135], v[0:3]\n\t"
    "buffer_load_dwordx4 a[96:99], %[vtok], %[stok], s50 offen offset:0 sc1\n\t"
    "v_mfma_f32_16x16x32_bf16 v[4:7], a[0:3], a[136:139], v[4:7]\n\t"
    "ds_read_b64_tr_b16 a[16:17], %[vt] offset:32768\n\t"
    "v_mfma_f32_16x16x32_bf16 v[4:7], a[4:7], a[140:143], v[4:7]\n\t"
    "v_mfma_f32_16x16x32_bf16 v[8:11], a[0:3], a[144:147], v[8:11]\n\t"
    "ds_read_b64_tr_b16 a[18:19], %[vt] offset:33792\n\t"
    "v_mfma_f32_16x16x32_bf16 v[8:11], a[4:7], a[148:151], v[8:11]\n\t"
    "ds_read_b64_tr_b16 a[20:21], %[vt] offset:40960\n\t"
    "v_mfma_f32_16x16x32_bf16 v[12:15], a[0:3], a[152:155], v[12:15]\n\t"
    "ds_read_b64_tr_b16 a[22:23], %[vt] offset:41984\n\t"
    "v_mfma_f32_16x16x32_bf16 v[12:15], a[4:7], a[156:159], v[12:15]\n\t"
    "s_waitcnt lgkmcnt(4)\n\t"
    "v_mfma_f32_16x16x32_bf16 v[16:19], a[8:11], a[128:131], v[16:19]\n\t"
    "v_xor_b32 %[vt], 96, %[vtrB]\n\t"
    "v_mfma_f32_16x16x32_bf16 v[16:19], a[12:15], a[132:135], v[16:19]\n\t"
    "buffer_load_dwordx4 %[vdma2], %[sw], s43 offen offset:2048 nt lds\n\t"
    "v_mfma_f32_16x16x32_bf16 v[20:23], a[8:11], a[136:139], v[20:23]\n\t"
    "ds_read_b64_tr_b16 a[24:25], %[vt] offset:32768\n\t"
    "v_mfma_f32_16x16x32_bf16 v[20:23], a[12:15], a[140:143], v[20:23]\n\t"
    "v_mfma_f32_16x16x32_bf16 v[24:27], a[8:11], a[144:147], v[24:27]\n\t"
    "ds_read_b64_tr_b16 a[26:27], %[vt] offset:33792\n\t"
    "v_mfma_f32_16x16x32_bf16 v[24:27], a[12:15], a[148:151], v[24:27]\n\t"
    "ds_read_b64_tr_b16 a[28:29], %[vt] offset:40960\n\t"
    "v_mfma_f32_16x16x32_bf16 v[28:31], a[8:11], a[152:155], v[28:31]\n\t"
    "ds_read_b64_tr_b16 a[30:31], %[vt] offset:41984\n\t"
    "v_mfma_f32_16x16x32_bf16 v[28:31], a[12:15], a[156:159], v[28:31]\n\t"
    "s_waitcnt lgkmcnt(4)\n\t"
    "v_mfma_f32_16x16x32_bf16 v[32:35], a[16:19], a[128:131], v[32:35]\n\t"
    "v_xor_b32 %[vt], 128, %[vtrB]\n\t"
    "v_mfma_f32_16x16x32_bf16 v[32:35], a[20:23], a[132:135], v[32:35]\n\t"
    "buffer_load_dwordx4 a[100:103], %[vtok], %[stok], s51 offen offset:0 sc1\n\t"
    "v_mfma_f32_16x16x32_bf16 v[36:39], a[16:19], a[136:139], v[36:39]\n\t"
    "ds_read_b64_tr_b16 a[0:1], %[vt] offset:32768\n\t"
    "v_mfma_f32_16x16x32_bf16 v[36:39], a[20:23], a[140:143], v[36:39]\n\t"
    "v_mfma_f32_16x16x32_bf16 v[40:43], a[16:19], a[144:147], v[40:43]\n\t"
    "ds_read_b64_tr_b16 a[2:3], %[vt] offset:33792\n\t"
    "v_mfma_f32_16x16x32_bf16 v[40:43], a[20:23], a[148:151], v[40:43]\n\t"
    "ds_read_b64_tr_b16 a[4:5], %[vt] offset:40960\n\t"
    "v_mfma_f32_16x16x32_bf16 v[44:47], a[16:19], a[152:155], v[44:47]\n\t"
    "ds_read_b64_tr_b16 a[6:7], %[vt] offset:41984\n\t"
    "v_mfma_f32_16x16x32_bf16 v[44:47], a[20:23], a[156:159], v[44:47]\n\t"
    "s_waitcnt lgkmcnt(4)\n\t"
    "v_mfma_f32_16x16x32_bf16 v[48:51], a[24:27], a[128:131], v[48:51]\n\t"
    "v_xor_b32 %[vt], 160, %[vtrB]\n\t"
    "v_mfma_f32_16x16x32_bf16 v[48:51], a[28:31], a[132:135], v[48:51]\n\t"
    "buffer_load_dwordx4 %[vdma3], %[sw], s43 offen offset:3072 nt lds\n\t"
    "v_mfma_f32_16x16x32_bf16 v[52:55], a[24:27], a[136:139], v[52:55]\n\t"
    "ds_read_b64_tr_b16 a[8:9], %[vt] offset:32768\n\t"
    "v_mfma_f32_16x16x32_bf16 v[52:55], a[28:31], a[140:143], v[52:55]\n\t"
    "v_mfma_f32_16x16x32_bf16 v[56:59], a[24:27], a[144:147], v[56:59]\n\t"
    "ds_read_b64_tr_b16 a[10:11], %[vt] offset:33792\n\t"
    "v_mfma_f32_16x16x32_bf16 v[56:59], a[28:31], a[148:151], v[56:59]\n\t"
    "ds_read_b64_tr_b16 a[12:13], %[vt] offset:40960\n\t"
    "v_mfma_f32_16x16x32_bf16 v[60:63], a[24:27], a[152:155], v[60:63]\n\t"
    "ds_read_b64_tr_b16 a[14:15], %[vt] offset:41984\n\t"
    "v_mfma_f32_16x16x32_bf16 v[60:63], a[28:31], a[156:159], v[60:63]\n\t"
    "s_waitcnt lgkmcnt(4)\n\t"
    "v_mfma_f32_16x16x32_bf16 v[64:67], a[0:3], a[128:131], v[64:67]\n\t"
    "v_xor_b32 %[vt], 192, %[vtrB]\n\t"
    "v_mfma_f32_16x16x32_bf16 v[64:67], a[4:7], a[132:135], v[64:67]\n\t"
    "buffer_load_dwordx4 a[104:107], %[vtok], %[stok], s50 offen offset:1024 sc1\n\t"
    "v_mfma_f32_16x16x32_bf16 v[68:71], a[0:3], a[136:139], v[68:71]\n\t"
    "ds_read_b64_tr_b16 a[16:17], %[vt] offset:32768\n\t"
    "v_mfma_f32_16x16x32_bf16 v[68:71], a[4:7], a[140:143], v[68:71]\n\t"
    "v_mfma_f32_16x16x32_bf16 v[72:75], a[0:3], a[144:147], v[72:75]\n\t"
    "ds_read_b64_tr_b16 a[18:19], %[vt] offset:33792\n\t"
    "v_mfma_f32_16x16x32_bf16 v[72:75], a[4:7], a[148:151], v[72:75]\n\t"
    "ds_read_b64_tr_b16 a[20:21], %[vt] offset:40960\n\t"
    "v_mfma_f32_16x16x32_bf16 v[76:79], a[0:3], a[152:155], v[76:79]\n\t"
    "ds_read_b64_tr_b16 a[22:23], %[vt] offset:41984\n\t"
    "v_mfma_f32_16x16x32_bf16 v[76:79], a[4:7], a[156:159], v[76:79]\n\t"
    "s_waitcnt lgkmcnt(4)\n\t"
    "v_mfma_f32_16x16x32_bf16 v[80:83], a[8:11], a[128:131], v[80:83]\n\t"
    "v_xor_b32 %[vt], 224, %[vtrB]\n\t"
    "v_mfma_f32_16x16x32_bf16 v[80:83], a[12:15], a[132:135], v[80:83]\n\t"
    "s_add_u32 m0, %[sm0], 69632\n\t"
    "s_add_u32 s43, s42, 114688\n\t"
    "buffer_load_dwordx4 %[vdma0], %[sw], s43 offen offset:0 nt lds\n\t"
    "v_mfma_f32_16x16x32_bf16 v[84:87], a[8:11], a[136:139], v[84:87]\n\t"
    "ds_read_b64_tr_b16 a[24:25], %[vt] offset:32768\n\t"
    "v_mfma_f32_16x16x32_bf16 v[84:87], a[12:15], a[140:143], v[84:87]\n\t"
    "v_mfma_f32_16x16x32_bf16 v[88:91], a[8:11], a[144:147], v[88:91]\n\t"
    "ds_read_b64_tr_b16 a[26:27], %[vt] offset:33792\n\t"
    "v_mfma_f32_16x16x32_bf16 v[88:91], a[12:15], a[148:151], v[88:91]\n\t"
    "ds_read_b64_tr_b16 a[28:29], %[vt] offset:40960\n\t"
    "v_mfma_f32_16x16x32_bf16 v[92:95], a[8:11], a[152:155], v[92:95]\n\t"
    "ds_read_b64_tr_b16 a[30:31], %[vt] offset:41984\n\t"
    "v_mfma_f32_16x16x32_bf16 v[92:95], a[12:15], a[156:159], v[92:95]\n\t"
    "s_waitcnt lgkmcnt(4)\n\t"
    "v_mfma_f32_16x16x32_bf16 v[96:99], a[16:19], a[128:131], v[96:99]\n\t"
    "ds_read_b64_tr_b16 a[0:1], %[vtrB] offset:49152\n\t"
    "v_mfma_f32_16x16x32_bf16 v[96:99], a[20:23], a[132:135], v[96:99]\n\t"
    "buffer_load_dwordx4 a[108:111], %[vtok], %[stok], s51 offen offset:1024 sc1\n\t"
    "v_mfma_f32_16x16x32_bf16 v[100:103], a[16:19], a[136:139], v[100:103]\n\t"
    "v_mfma_f32_16x16x32_bf16 v[100:103], a[20:23], a[140:143], v[100:103]\n\t"
    "ds_read_b64_tr_b16 a[2:3], %[vtrB] offset:50176\n\t"
    "v_mfma_f32_16x16x32_bf16 v[104:107], a[16:19], a[144:147], v[104:107]\n\t"
    "ds_read_b64_tr_b16 a[4:5], %[vtrB] offset:57344\n\t"
    "v_mfma_f32_16x16x32_bf16 v[104:107], a[20:23], a[148:151], v[104:107]\n\t"
    "v_mfma_f32_16x16x32_bf16 v[108:111], a[16:19], a[152:155], v[108:111]\n\t"
    "ds_read_b64_tr_b16 a[6:7], %[vtrB] offset:58368\n\t"
    "v_mfma_f32_16x16x32_bf16 v[108:111], a[20:23], a[156:159], v[108:111]\n\t"
    "s_waitcnt lgkmcnt(4)\n\t"
    "v_mfma_f32_16x16x32_bf16 v[112:115], a[24:27], a[128:131], v[112:115]\n\t"
    "v_xor_b32 %[vt], 32, %[vtrB]\n\t"
    "v_mfma_f32_16x16x32_bf16 v[112:115], a[28:31], a[132:135], v[112:115]\n\t"
    "buffer_load_dwordx4 a[112:115], %[vtok], %[stok], s50 offen offset:2048 sc1\n\t"
    "v_mfma_f32_16x16x32_bf16 v[116:119], a[24:27], a[136:139], v[116:119]\n\t"
    "ds_read_b64_tr_b16 a[8:9], %[vt] offset:49152\n\t"
    "v_mfma_f32_16x16x32_bf16 v[116:119], a[28:31], a[140:143], v[116:119]\n\t"
    "v_mfma_f32_16x16x32_bf16 v[120:123], a[24:27], a[144:147], v[120:123]\n\t"
    "ds_read_b64_tr_b16 a[10:11], %[vt] offset:50176\n\t"
    "v_mfma_f32_16x16x32_bf16 v[120:123], a[28:31], a[148:151], v[120:123]\n\t"
    "ds_read_b64_tr_b16 a[12:13], %[vt] offset:57344\n\t"
    "v_mfma_f32_16x16x32_bf16 v[124:127], a[24:27], a[152:155], v[124:127]\n\t"
    "ds_read_b64_tr_b16 a[14:15], %[vt] offset:58368\n\t"
    "v_mfma_f32_16x16x32_bf16 v[124:127], a[28:31], a[156:159], v[124:127]\n\t"
    "s_waitcnt lgkmcnt(4)\n\t"
    "v_mfma_f32_16x16x32_bf16 v[128:131], a[0:3], a[128:131], v[128:131]\n\t"
    "v_xor_b32 %[vt], 64, %[vtrB]\n\t"
    "v_mfma_f32_16x16x32_bf16 v[128:131], a[4:7], a[132:135], v[128:131]\n\t"
    "buffer_load_dwordx4 %[vdma1], %[sw], s43 offen offset:1024 nt lds\n\t"
    "v_mfma_f32_16x16x32_bf16 v[132:135], a[0:3], a[136:139], v[132:135]\n\t"
    "ds_read_b64_tr_b16 a[16:17], %[vt] offset:49152\n\t"
    "v_mfma_f32_16x16x32_bf16 v[132:135], a[4:7], a[140:143], v[132:135]\n\t"
    "v_mfma_f32_16x16x32_bf16 v[136:139], a[0:3], a[144:147], v[136:139]\n\t"
    "ds_read_b64_tr_b16 a[18:19], %[vt] offset:50176\n\t"
    "v_mfma_f32_16x16x32_bf16 v[136:139], a[4:7], a[148:151], v[136:139]\n\t"
    "ds_read_b64_tr_b16 a[20:21], %[vt] offset:57344\n\t"
    "v_mfma_f32_16x16x32_bf16 v[140:143], a[0:3], a[152:155], v[140:143]\n\t"
    "ds_read_b64_tr_b16 a[22:23], %[vt] offset:58368\n\t"
    "v_mfma_f32_16x16x32_bf16 v[140:143], a[4:7], a[156:159], v[140:143]\n\t"
    "s_waitcnt lgkmcnt(4)\n\t"
    "v_mfma_f32_16x16x32_bf16 v[144:147], a[8:11], a[128:131], v[144:147]\n\t"
    "v_xor_b32 %[vt], 96, %[vtrB]\n\t"
    "v_mfma_f32_16x16x32_bf16 v[144:147], a[12:15], a[132:135], v[144:147]\n\t"
    "buffer_load_dwordx4 a[116:119], %[vtok], %[stok], s51 offen offset:2048 sc1\n\t"
    "v_mfma_f32_16x16x32_bf16 v[148:151], a[8:11], a[136:139], v[148:151]\n\t"
    "ds_read_b64_tr_b16 a[24:25], %[vt] offset:49152\n\t"
    "v_mfma_f32_16x16x32_bf16 v[148:151], a[12:15], a[140:143], v[148:151]\n\t"
    "v_mfma_f32_16x16x32_bf16 v[152:155], a[8:11], a[144:147], v[152:155]\n\t"
    "ds_read_b64_tr_b16 a[26:27], %[vt] offset:50176\n\t"
    "v_mfma_f32_16x16x32_bf16 v[152:155], a[12:15], a[148:151], v[152:155]\n\t"
    "ds_read_b64_tr_b16 a[28:29], %[vt] offset:57344\n\t"
    "v_mfma_f32_16x16x32_bf16 v[156:159], a[8:11], a[152:155], v[156:159]\n\t"
    "ds_read_b64_tr_b16 a[30:31], %[vt] offset:58368\n\t"
    "v_mfma_f32_16x16x32_bf16 v[156:159], a[12:15], a[156:159], v[156:159]\n\t"
    "s_waitcnt lgkmcnt(4)\n\t"
    "v_mfma_f32_16x16x32_bf16 v[160:163], a[16:19], a[128:131], v[160:163]\n\t"
    "v_xor_b32 %[vt], 128, %[vtrB]\n\t"
    "v_mfma_f32_16x16x32_bf16 v[160:163], a[20:23], a[132:135], v[160:163]\n\t"
    "buffer_load_dwordx4 %[vdma2], %[sw], s43 offen offset:2048 nt lds\n\t"
    "v_mfma_f32_16x16x32_bf16 v[164:167], a[16:19], a[136:139], v[164:167]\n\t"
    "ds_read_b64_tr_b16 a[0:1], %[vt] offset:49152\n\t"
    "v_mfma_f32_16x16x32_bf16 v[164:167], a[20:23], a[140:143], v[164:167]\n\t"
    "v_mfma_f32_16x16x32_bf16 v[168:171], a[16:19], a[144:147], v[168:171]\n\t"
    "ds_read_b64_tr_b16 a[2:3], %[vt] offset:50176\n\t"
    "v_mfma_f32_16x16x32_bf16 v[168:171], a[20:23], a[148:151], v[168:171]\n\t"
    "ds_read_b64_tr_b16 a[4:5], %[vt] offset:57344\n\t"
    "v_mfma_f32_16x16x32_bf16 v[172:175], a[16:19], a[152:155], v[172:175]\n\t"
    "ds_read_b64_tr_b16 a[6:7], %[vt] offset:58368\n\t"
    "v_mfma_f32_16x16x32_bf16 v[172:175], a[20:23], a[156:159], v[172:175]\n\t"
    "s_waitcnt lgkmcnt(4)\n\t"
    "v_mfma_f32_16x16x32_bf16 v[176:179], a[24:27], a[128:131], v[176:179]\n\t"
    "v_xor_b32 %[vt], 160, %[vtrB]\n\t"
    "v_mfma_f32_16x16x32_bf16 v[176:179], a[28:31], a[132:135], v[176:179]\n\t"
    "buffer_load_dwordx4 a[120:123], %[vtok], %[stok], s50 offen offset:3072 sc1\n\t"
    "v_mfma_f32_16x16x32_bf16 v[180:183], a[24:27], a[136:139], v[180:183]\n\t"
    "ds_read_b64_tr_b16 a[8:9], %[vt] offset:49152\n\t"
    "v_mfma_f32_16x16x32_bf16 v[180:183], a[28:31], a[140:143], v[180:183]\n\t"
    "v_mfma_f32_16x16x32_bf16 v[184:187], a[24:27], a[144:147], v[184:187]\n\t"
    "ds_read_b64_tr_b16 a[10:11], %[vt] offset:50176\n\t"
    "v_mfma_f32_16x16x32_bf16 v[184:187], a[28:31], a[148:151], v[184:187]\n\t"
    "ds_read_b64_tr_b16 a[12:13], %[vt] offset:57344\n\t"
    "v_mfma_f32_16x16x32_bf16 v[188:191], a[24:27], a[152:155], v[188:191]\n\t"
    "ds_read_b64_tr_b16 a[14:15], %[vt] offset:58368\n\t"
    "v_mfma_f32_16x16x32_bf16 v[188:191], a[28:31], a[156:159], v[188:191]\n\t"
    "s_waitcnt lgkmcnt(4)\n\t"
    "v_mfma_f32_16x16x32_bf16 v[192:195], a[0:3], a[128:131], v[192:195]\n\t"
    "v_xor_b32 %[vt], 192, %[vtrB]\n\t"
    "buffer_load_dwordx4 %[vdma3], %[sw], s43 offen offset:3072 nt lds\n\t"
    "s_add_u32 s42, s42, 458752\n\t"
    "v_mfma_f32_16x16x32_bf16 v[192:195], a[4:7], a[132:135], v[192:195]\n\t"
    "ds_read_b64_tr_b16 a[16:17], %[vt] offset:49152\n\t"
    "v_mfma_f32_16x16x32_bf16 v[196:199], a[0:3], a[136:139], v[196:199]\n\t"
    "ds_read_b64_tr_b16 a[18:19], %[vt] offset:50176\n\t"
    "ds_read_b64_tr_b16 a[20:21], %[vt] offset:57344\n\t"
    "v_mfma_f32_16x16x32_bf16 v[196:199], a[4:7], a[140:143], v[196:199]\n\t"
    "ds_read_b64_tr_b16 a[22:23], %[vt] offset:58368\n\t"
    "v_mfma_f32_16x16x32_bf16 v[200:203], a[0:3], a[144:147], v[200:203]\n\t"
    "v_xor_b32 %[vt], 224, %[vtrB]\n\t"
    "v_mfma_f32_16x16x32_bf16 v[200:203], a[4:7], a[148:151], v[200:203]\n\t"
    "ds_read_b64_tr_b16 a[24:25], %[vt] offset:49152\n\t"
    "ds_read_b64_tr_b16 a[26:27], %[vt] offset:50176\n\t"
    "v_mfma_f32_16x16x32_bf16 v[204:207], a[0:3], a[152:155], v[204:207]\n\t"
    "ds_read_b64_tr_b16 a[28:29], %[vt] offset:57344\n\t"
    "v_mfma_f32_16x16x32_bf16 v[204:207], a[4:7], a[156:159], v[204:207]\n\t"
    "ds_read_b64_tr_b16 a[30:31], %[vt] offset:58368\n\t"
    "s_waitcnt lgkmcnt(8)\n\t"
    "v_mfma_f32_16x16x32_bf16 v[208:211], a[8:11], a[128:131], v[208:211]\n\t"
    "buffer_load_dwordx4 a[124:127], %[vtok], %[stok], s51 offen offset:3072 sc1\n\t"
    "s_add_u32 s50, s50, 262144\n\t"
    "s_add_u32 s51, s51, 262144\n\t"
    "v_mfma_f32_16x16x32_bf16 v[208:211], a[12:15], a[132:135], v[208:211]\n\t"
    "v_mfma_f32_16x16x32_bf16 v[212:215], a[8:11], a[136:139], v[212:215]\n\t"
    "v_mfma_f32_16x16x32_bf16 v[212:215], a[12:15], a[140:143], v[212:215]\n\t"
    "v_mfma_f32_16x16x32_bf16 v[216:219], a[8:11], a[144:147], v[216:219]\n\t"
    "v_mfma_f32_16x16x32_bf16 v[216:219], a[12:15], a[148:151], v[216:219]\n\t"
    "v_mfma_f32_16x16x32_bf16 v[220:223], a[8:11], a[152:155], v[220:223]\n\t"
    "v_mfma_f32_16x16x32_bf16 v[220:223], a[12:15], a[156:159], v[220:223]\n\t"
    "s_waitcnt vmcnt(32) lgkmcnt(0)\n\t"
    "s_barrier\n\t"
    "v_mfma_f32_16x16x32_bf16 v[224:227], a[16:19], a[128:131], v[224:227]\n\t"
    "ds_read_b64_tr_b16 a[0:1], %[vtrA] offset:0\n\t"
    "v_mfma_f32_16x16x32_bf16 v[224:227], a[20:23], a[132:135], v[224:227]\n\t"
    "ds_read_b64_tr_b16 a[2:3], %[vtrA] offset:1024\n\t"
    "v_mfma_f32_16x16x32_bf16 v[228:231], a[16:19], a[136:139], v[228:231]\n\t"
    "v_mfma_f32_16x16x32_bf16 v[228:231], a[20:23], a[140:143], v[228:231]\n\t"
    "ds_read_b64_tr_b16 a[4:5], %[vtrA] offset:8192\n\t"
    "v_mfma_f32_16x16x32_bf16 v[232:235], a[16:19], a[144:147], v[232:235]\n\t"
    "ds_read_b64_tr_b16 a[6:7], %[vtrA] offset:9216\n\t"
    "v_mfma_f32_16x16x32_bf16 v[232:235], a[20:23], a[148:151], v[232:235]\n\t"
    "v_mfma_f32_16x16x32_bf16 v[236:239], a[16:19], a[152:155], v[236:239]\n\t"
    "s_add_u32 m0, %[sm0], 98304\n\t"
    "s_add_u32 s43, s42, 0\n\t"
    "buffer_load_dwordx4 %[vdma0], %[sw], s43 offen offset:0 nt lds\n\t"
    "v_mfma_f32_16x16x32_bf16 v[236:239], a[20:23], a[156:159], v[236:239]\n\t"
    "v_mfma_f32_16x16x32_bf16 a[160:163], a[24:27], a[128:131], a[160:163]\n\t"
    "v_xor_b32 %[vt], 32, %[vtrA]\n\t"
    "v_mfma_f32_16x16x32_bf16 a[160:163], a[28:31], a[132:135], a[160:163]\n\t"
    "ds_read_b64_tr_b16 a[8:9], %[vt] offset:0\n\t"
    "v_mfma_f32_16x16x32_bf16 a[164:167], a[24:27], a[136:139], a[164:167]\n\t"
    "ds_read_b64_tr_b16 a[10:11], %[vt] offset:1024\n\t"
    "v_mfma_f32_16x16x32_bf16 a[164:167], a[28:31], a[140:143], a[164:167]\n\t"
    "v_mfma_f32_16x16x32_bf16 a[168:171], a[24:27], a[144:147], a[168:171]\n\t"
    "ds_read_b64_tr_b16 a[12:13], %[vt] offset:8192\n\t"
    "v_mfma_f32_16x16x32_bf16 a[168:171], a[28:31], a[148:151], a[168:171]\n\t"
    "ds_read_b64_tr_b16 a[14:15], %[vt] offset:9216\n\t"
    "v_mfma_f32_16x16x32_bf16 a[172:175], a[24:27], a[152:155], a[172:175]\n\t"
    "buffer_load_dwordx4 %[vdma1], %[sw], s43 offen offset:1024 nt lds\n\t"
    "v_mfma_f32_16x16x32_bf16 a[172:175], a[28:31], a[156:159], a[172:175]\n\t"
    "s_sub_u32 s45, s45, 1\n\t"
    "s_cmp_lg_u32 s45, 0\n\t"
    "s_cbranch_scc1 L_g1_loop_%=\n\t"
    "L_g1_done_%=:\n\t"
    "s_waitcnt vmcnt(0) lgkmcnt(0)\n\t"
    "s_barrier\n\t"
    "s_nop 7\n\t"
      : "=&{v[0:15]}"(a0), "=&{v[16:31]}"(a1), "=&{v[32:47]}"(a2), "=&{v[48:63]}"(a3), "=&{v[64:79]}"(a4), "=&{v[80:95]}"(a5), "=&{v[96:111]}"(a6), "=&{v[112:127]}"(a7),
        "=&{v[128:143]}"(a8), "=&{v[144:159]}"(a9), "=&{v[160:175]}"(a10), "=&{v[176:191]}"(a11), "=&{v[192:207]}"(a12), "=&{v[208:223]}"(a13), "=&{v[224:239]}"(a14), "=&{a[160:175]}"(a15),
        [vt] "=&v"(vt), [vt2] "=&v"(vt2)
      : [vtrA] "v"(vtrA), [vtrB] "v"(vtrB), [vtok] "v"(vtok), [vdma0] "v"(vdma0), [vdma1] "v"(vdma1), [vdma2] "v"(vdma2), [vdma3] "v"(vdma3), [stok] "s"(stok), [sw] "s"(sw), [sm0] "s"(sm0), [flag] "s"(flag), [want] "s"(want), [wid] "s"(w), [vpoll] "v"(vpoll)
      : "memory", "scc", "vcc", "a0", "a1", "a2", "a3", "a4", "a5", "a6", "a7", "a8", "a9", "a10", "a11", "a12", "a13", "a14", "a15", "a16", "a17", "a18", "a19", "a20", "a21", "a22", "a23", "a24", "a25", "a26", "a27", "a28", "a29", "a30", "a31", "a32", "a33", "a34", "a35", "a36", "a37", "a38", "a39", "a40", "a41", "a42", "a43", "a44", "a45", "a46", "a47", "a48", "a49", "a50", "a51", "a52", "a53", "a54", "a55", "a56", "a57", "a58", "a59", "a60", "a61", "a62", "a63", "a64", "a65", "a66", "a67", "a68", "a69", "a70", "a71", "a72", "a73", "a74", "a75", "a76", "a77", "a78", "a79", "a80", "a81", "a82", "a83", "a84", "a85", "a86", "a87", "a88", "a89", "a90", "a91", "a92", "a93", "a94", "a95", "a96", "a97", "a98", "a99", "a100", "a101", "a102", "a103", "a104", "a105", "a106", "a107", "a108", "a109", "a110", "a111", "a112", "a113", "a114", "a115", "a116", "a117", "a118", "a119", "a120", "a121", "a122", "a123", "a124", "a125", "a126", "a127", "a128", "a129", "a130", "a131", "a132", "a133", "a134", "a135", "a136", "a137", "a138", "a139", "a140", "a141", "a142", "a143", "a144", "a145", "a146", "a147", "a148", "a149", "a150", "a151", "a152", "a153", "a154", "a155", "a156", "a157", "a158", "a159", "s42", "s43", "s44", "s45", "s46", "s47", "s48", "s49", "s50", "s51", "s52", "s53", "s54", "s55", "s56", "s57", "s58", "s59", "s60", "s61");
    G2_PREFETCH();
    char* ob = (char*)P.hm + (size_t)t16 * 1024 + (size_t)pn1 * 4 * 131072 + lane * 16;
    f32x2 C1 = {-2.302208198f, -2.302208198f}, C3 = {-0.1029432397f, -0.1029432397f}, ONE = {1.f, 1.f};
    asm volatile("" : "+v"(C1), "+v"(C3), "+v"(ONE));
    auto h4 = [&](const f32x16& ga, const f32x16& up, int m) -> u32x2 {
      return (u32x2){geglu2((f32x2){ga[4 * m], ga[4 * m + 1]}, (f32x2){up[4 * m], up[4 * m + 1]}, C1, C3, ONE),
                     geglu2((f32x2){ga[4 * m + 2], ga[4 * m + 3]}, (f32x2){up[4 * m + 2], up[4 * m + 3]}, C1, C3, ONE)};
    };
    auto st2 = [&](const f32x16& g0, const f32x16& u0, const f32x16& g1, const f32x16& u1, int kap) {
#pragma unroll
      for (int m = 0; m < 4; m++) { const u32x2 x = h4(g0, u0, m), y = h4(g1, u1, m); st16_dev(ob + (size_t)kap * 131072 + m * 1024, (u32x4){x[0], x[1], y[0], y[1]}); }
    };
    st2(a6, a14, a7, a15, 3); st2(a0, a8, a1, a9, 0); st2(a2, a10, a3, a11, 1); st2(a4, a12, a5, a13, 2);
    drain_and_barrier();
    arrive(P.flags + FL_B4 + ((pm1 * 2 + (pn1 >= 14 ? 1 : 0)) * 2 + (pn1 & 1)) * 32);
  } else {
    G2_PREFETCH();
    const int pi = xcd * 4 + (li - 28);
    const u32x4* src = (const u32x4*)P.wd + (size_t)pi * (F_ * D_ * 2 / 16 / 32) + tid;
    u32 acc = 0;
#pragma unroll 1
    for (int i = 0; i < F_ * D_ * 2 / 16 / 32 / 256; i += 14) {
      u32x4 v[14];
#pragma unroll
      for (int j = 0; j < 14; j++) v[j] = src[(i + j) * 256];
#pragma unroll
      for (int j = 0; j < 14; j++) acc ^= v[j][0] ^ v[j][3];
    }
    asm volatile("" :: "v"(acc));
  }
  {
    const int kh = xcd & 1;
    const int pm = (xcd >> 2) * 8 + (li & 7), pn = ((xcd >> 1) & 1) * 4 + (li >> 3);
    const u32* flag = P.flags + FL_B4 + ((pm >> 1) * 2 + kh) * 64; const u32 want = 7u, vpoll = (lane & 1) * 128;
    const u32 vtrA = ldsb + g * 1024 + qq * 256 + 8 * p4 + 32 * (qq + 4 * (g & 1));
    const u32 vtok = lane * 16 + (pm * 8 + w * 2) * 1024;
    const u32 drow = (u32)((16 * w + (lane >> 4)) * 2048 + pn * 256);
    const u32 vdma0 = drow + dsw0, vdma1 = drow + dsw1 + 7168, vdma2 = vdma0 + 2 * 7168, vdma3 = vdma1 + 2 * 7168;
    const __amdgpu_buffer_rsrc_t stok = __builtin_amdgcn_make_buffer_rsrc((void*)((const char*)P.hm + (size_t)kh * 56 * 131072), (short)0, 56 * 131072, 0x00020000);
    const __amdgpu_buffer_rsrc_t sw = __builtin_amdgcn_make_buffer_rsrc((void*)((const char*)P.wd + (size_t)kh * 1792 * 2048), (short)0, 1792 * 2048, 0x00020000);
    const u32 sm0 = rfl(ldsb + w * 4096);
    f32x16 a0, a1, a2, a3; u32 vt, vt2;
    asm volatile(
    "s_mov_b32 s50, 0\n\t"
    "s_mov_b32 s51, 131072\n\t"
    "s_mov_b32 s42, 393216\n\t"
    "v_xor_b32 v128, 0, %[vtrA]\n\t"
    "v_xor_b32 v129, 32, %[vtrA]\n\t"
    "v_xor_b32 v130, 64, %[vtrA]\n\t"
    "v_xor_b32 v131, 96, %[vtrA]\n\t"
    "v_xor_b32 v132, 128, %[vtrA]\n\t"
    "v_xor_b32 v133, 160, %[vtrA]\n\t"
    "v_xor_b32 v134, 192, %[vtrA]\n\t"
    "v_xor_b32 v135, 224, %[vtrA]\n\t"
    "v_accvgpr_write_b32 a0, 0\n\t"
    "v_accvgpr_write_b32 a1, 0\n\t"
    "v_accvgpr_write_b32 a2, 0\n\t"
    "v_accvgpr_write_b32 a3, 0\n\t"
    "s_nop 1\n\t"
    "v_mfma_f32_32x32x16_bf16 v[0:15], a[0:3], a[0:3], 0\n\t"
    "v_mfma_f32_32x32x16_bf16 v[16:31], a[0:3], a[0:3], 0\n\t"
    "v_mfma_f32_32x32x16_bf16 v[32:47], a[0:3], a[0:3], 0\n\t"
    "v_mfma_f32_32x32x16_bf16 v[48:63], a[0:3], a[0:3], 0\n\t"
    "s_cmp_lg_u32 %[wid], 0\n\t"
    "s_cbranch_scc1 L_g2_go_%=\n\t"
    "s_mov_b32 s44, 0x20000\n\t"
    "L_g2_poll_%=:\n\t"
    "global_load_dword %[vt2], %[vpoll], %[flag] sc1\n\t"
    "s_waitcnt vmcnt(0)\n\t"
    "v_cmp_gt_u32 vcc, %[want], %[vt2]\n\t"
    "s_cbranch_vccz L_g2_go_%=\n\t"
    "s_sleep 1\n\t"
    "s_sub_u32 s44, s44, 1\n\t"
    "s_cmp_lg_u32 s44, 0\n\t"
    "s_cbranch_scc1 L_g2_poll_%=\n\t"
    "L_g2_go_%=:\n\t"
    "s_barrier\n\t"
    "buffer_load_dwordx4 a[32:35], %[vtok], %[stok], s50 offen offset:0 sc1\n\t"
    "buffer_load_dwordx4 a[36:39], %[vtok], %[stok], s51 offen offset:0 sc1\n\t"
    "buffer_load_dwordx4 a[40:43], %[vtok], %[stok], s50 offen offset:1024 sc1\n\t"
    "buffer_load_dwordx4 a[44:47], %[vtok], %[stok], s51 offen offset:1024 sc1\n\t"
    "s_mov_b32 s54, 262144\n\t"
    "s_mov_b32 s55, 393216\n\t"
    "s_mov_b32 s56, 524288\n\t"
    "s_mov_b32 s57, 655360\n\t"
    "buffer_load_dwordx4 a[64:67], %[vtok], %[stok], s54 offen offset:0 sc1\n\t"
    "buffer_load_dwordx4 a[68:71], %[vtok], %[stok], s55 offen offset:0 sc1\n\t"
    "buffer_load_dwordx4 a[72:75], %[vtok], %[stok], s54 offen offset:1024 sc1\n\t"
    "buffer_load_dwordx4 a[76:79], %[vtok], %[stok], s55 offen offset:1024 sc1\n\t"
    "s_add_u32 s50, s50, 262144\n\t"
    "s_add_u32 s51, s51, 262144\n\t"
    "s_add_u32 s50, s50, 262144\n\t"
    "s_add_u32 s51, s51, 262144\n\t"
    "s_add_u32 s50, s50, 262144\n\t"
    "s_add_u32 s51, s51, 262144\n\t"
    "s_mov_b32 s45, 7\n\t"
    "s_waitcnt vmcnt(4)\n\t"
    "s_barrier\n\t"
    "ds_read_b64_tr_b16 a[0:1], v128 offset:0\n\t"
    "ds_read_b64_tr_b16 a[2:3], v128 offset:4096\n\t"
    "ds_read_b64_tr_b16 a[4:5], v128 offset:8192\n\t"
    "ds_read_b64_tr_b16 a[6:7], v128 offset:12288\n\t"
    "ds_read_b64_tr_b16 a[8:9], v129 offset:0\n\t"
    "ds_read_b64_tr_b16 a[10:11], v129 offset:4096\n\t"
    "ds_read_b64_tr_b16 a[12:13], v129 offset:8192\n\t"
    "ds_read_b64_tr_b16 a[14:15], v129 offset:12288\n\t"
    "s_add_u32 m0, %[sm0], 49152\n\t"
    "s_add_u32 s43, s42, 0\n\t"
    "buffer_load_dwordx4 %[vdma0], %[sw], s43 offen offset:0 lds\n\t"
    "buffer_load_dwordx4 %[vdma1], %[sw], s43 offen offset:1024 lds\n\t"
    "L_g2_loop_%=:\n\t"
    "s_waitcnt lgkmcnt(4)\n\t"
    "v_mfma_f32_16x16x32_bf16 v[0:3], a[0:3], a[32:35], v[0:3]\n\t"
    "s_cmp_lg_u32 s45, 7\n\t"
    "s_cbranch_scc1 L_g2_x0_%=\n\t"
    "buffer_load_dwordx4 a[96:99], %[vtok], %[stok], s56 offen offset:0 sc1\n\t"
    "L_g2_x0_%=:\n\t"
    "ds_read_b64_tr_b16 a[16:17], v130 offset:0\n\t"
    "v_mfma_f32_16x16x32_bf16 v[0:3], a[4:7], a[36:39], v[0:3]\n\t"
    "buffer_load_dwordx4 a[128:131], %[vtok], %[stok], s50 offen offset:0 sc1\n\t"
    "v_mfma_f32_16x16x32_bf16 v[4:7], a[0:3], a[40:43], v[4:7]\n\t"
    "ds_read_b64_tr_b16 a[18:19], v130 offset:4096\n\t"
    "ds_read_b64_tr_b16 a[20:21], v130 offset:8192\n\t"
    "v_mfma_f32_16x16x32_bf16 v[4:7], a[4:7], a[44:47], v[4:7]\n\t"
    "ds_read_b64_tr_b16 a[22:23], v130 offset:12288\n\t"
    "s_waitcnt lgkmcnt(4)\n\t"
    "v_mfma_f32_16x16x32_bf16 v[8:11], a[8:11], a[32:35], v[8:11]\n\t"
    "s_cmp_lg_u32 s45, 7\n\t"
    "s_cbranch_scc1 L_g2_x1_%=\n\t"
    "buffer_load_dwordx4 a[100:103], %[vtok], %[stok], s57 offen offset:0 sc1\n\t"
    "L_g2_x1_%=:\n\t"
    "ds_read_b64_tr_b16 a[24:25], v131 offset:0\n\t"
    "v_mfma_f32_16x16x32_bf16 v[8:11], a[12:15], a[36:39], v[8:11]\n\t"
    "buffer_load_dwordx4 %[vdma2], %[sw], s43 offen offset:2048 lds\n\t"
    "v_mfma_f32_16x16x32_bf16 v[12:15], a[8:11], a[40:43], v[12:15]\n\t"
    "ds_read_b64_tr_b16 a[26:27], v131 offset:4096\n\t"
    "ds_read_b64_tr_b16 a[28:29], v131 offset:8192\n\t"
    "v_mfma_f32_16x16x32_bf16 v[12:15], a[12:15], a[44:47], v[12:15]\n\t"
    "ds_read_b64_tr_b16 a[30:31], v131 offset:12288\n\t"
    "s_waitcnt lgkmcnt(4)\n\t"
    "v_mfma_f32_16x16x32_bf16 v[16:19], a[16:19], a[32:35], v[16:19]\n\t"
    "s_cmp_lg_u32 s45, 7\n\t"
    "s_cbranch_scc1 L_g2_x2_%=\n\t"
    "buffer_load_dwordx4 a[104:107], %[vtok], %[stok], s56 offen offset:1024 sc1\n\t"
    "L_g2_x2_%=:\n\t"
    "ds_read_b64_tr_b16 a[0:1], v132 offset:0\n\t"
    "v_mfma_f32_16x16x32_bf16 v[16:19], a[20:23], a[36:39], v[16:19]\n\t"
    "buffer_load_dwordx4 a[132:135], %[vtok], %[stok], s51 offen offset:0 sc1\n\t"
    "v_mfma_f32_16x16x32_bf16 v[20:23], a[16:19], a[40:43], v[20:23]\n\t"
    "ds_read_b64_tr_b16 a[2:3], v132 offset:4096\n\t"
    "ds_read_b64_tr_b16 a[4:5], v132 offset:8192\n\t"
    "v_mfma_f32_16x16x32_bf16 v[20:23], a[20:23], a[44:47], v[20:23]\n\t"
    "ds_read_b64_tr_b16 a[6:7], v132 offset:12288\n\t"
    "s_waitcnt lgkmcnt(4)\n\t"
    "v_mfma_f32_16x16x32_bf16 v[24:27], a[24:27], a[32:35], v[24:27]\n\t"
    "s_cmp_lg_u32 s45, 7\n\t"
    "s_cbranch_scc1 L_g2_x3_%=\n\t"
    "buffer_load_dwordx4 a[108:111], %[vtok], %[stok], s57 offen offset:1024 sc1\n\t"
    "L_g2_x3_%=:\n\t"
    "ds_read_b64_tr_b16 a[8:9], v133 offset:0\n\t"
    "v_mfma_f32_16x16x32_bf16 v[24:27], a[28:31], a[36:39], v[24:27]\n\t"
    "buffer_load_dwordx4 a[136:139], %[vtok], %[stok], s50 offen offset:1024 sc1\n\t"
    "v_mfma_f32_16x16x32_bf16 v[28:31], a[24:27], a[40:43], v[28:31]\n\t"
    "ds_read_b64_tr_b16 a[10:11], v133 offset:4096\n\t"
    "ds_read_b64_tr_b16 a[12:13], v133 offset:8192\n\t"
    "v_mfma_f32_16x16x32_bf16 v[28:31], a[28:31], a[44:47], v[28:31]\n\t"
    "ds_read_b64_tr_b16 a[14:15], v133 offset:12288\n\t"
    "s_waitcnt lgkmcnt(4)\n\t"
    "v_mfma_f32_16x16x32_bf16 v[32:35], a[0:3], a[32:35], v[32:35]\n\t"
    "ds_read_b64_tr_b16 a[16:17], v134 offset:0\n\t"
    "buffer_load_dwordx4 %[vdma3], %[sw], s43 offen offset:3072 lds\n\t"
    "s_add_u32 s42, s42, 131072\n\t"
    "ds_read_b64_tr_b16 a[18:19], v134 offset:4096\n\t"
    "v_mfma_f32_16x16x32_bf16 v[32:35], a[4:7], a[36:39], v[32:35]\n\t"
    "ds_read_b64_tr_b16 a[20:21], v134 offset:8192\n\t"
    "ds_read_b64_tr_b16 a[22:23], v134 offset:12288\n\t"
    "v_mfma_f32_16x16x32_bf16 v[36:39], a[0:3], a[40:43], v[36:39]\n\t"
    "ds_read_b64_tr_b16 a[24:25], v135 offset:0\n\t"
    "ds_read_b64_tr_b16 a[26:27], v135 offset:4096\n\t"
    "v_mfma_f32_16x16x32_bf16 v[36:39], a[4:7], a[44:47], v[36:39]\n\t"
    "ds_read_b64_tr_b16 a[28:29], v135 offset:8192\n\t"
    "ds_read_b64_tr_b16 a[30:31], v135 offset:12288\n\t"
    "s_waitcnt lgkmcnt(8)\n\t"
    "v_mfma_f32_16x16x32_bf16 v[40:43], a[8:11], a[32:35], v[40:43]\n\t"
    "buffer_load_dwordx4 a[140:143], %[vtok], %[stok], s51 offen offset:1024 sc1\n\t"
    "s_add_u32 s50, s50, 262144\n\t"
    "s_add_u32 s51, s51, 262144\n\t"
    "v_mfma_f32_16x16x32_bf16 v[40:43], a[12:15], a[36:39], v[40:43]\n\t"
    "v_mfma_f32_16x16x32_bf16 v[44:47], a[8:11], a[40:43], v[44:47]\n\t"
    "v_mfma_f32_16x16x32_bf16 v[44:47], a[12:15], a[44:47], v[44:47]\n\t"
    "s_cmp_lg_u32 s45, 7\n\t"
    "s_cbranch_scc1 L_g2_w0_%=\n\t"
    "s_waitcnt vmcnt(12)\n\t"
    "L_g2_w0_%=:\n\t"
    "s_waitcnt vmcnt(16) lgkmcnt(0)\n\t"
    "s_barrier\n\t"
    "v_mfma_f32_16x16x32_bf16 v[48:51], a[16:19], a[32:35], v[48:51]\n\t"
    "ds_read_b64_tr_b16 a[0:1], v128 offset:16384\n\t"
    "ds_read_b64_tr_b16 a[2:3], v128 offset:20480\n\t"
    "v_mfma_f32_16x16x32_bf16 v[48:51], a[20:23], a[36:39], v[48:51]\n\t"
    "ds_read_b64_tr_b16 a[4:5], v128 offset:24576\n\t"
    "v_mfma_f32_16x16x32_bf16 v[52:55], a[16:19], a[40:43], v[52:55]\n\t"
    "ds_read_b64_tr_b16 a[6:7], v128 offset:28672\n\t"
    "v_mfma_f32_16x16x32_bf16 v[52:55], a[20:23], a[44:47], v[52:55]\n\t"
    "s_add_u32 m0, %[sm0], 0\n\t"
    "s_add_u32 s43, s42, 0\n\t"
    "buffer_load_dwordx4 %[vdma0], %[sw], s43 offen offset:0 lds\n\t"
    "v_mfma_f32_16x16x32_bf16 v[56:59], a[24:27], a[32:35], v[56:59]\n\t"
    "ds_read_b64_tr_b16 a[8:9], v129 offset:16384\n\t"
    "ds_read_b64_tr_b16 a[10:11], v129 offset:20480\n\t"
    "v_mfma_f32_16x16x32_bf16 v[56:59], a[28:31], a[36:39], v[56:59]\n\t"
    "ds_read_b64_tr_b16 a[12:13], v129 offset:24576\n\t"
    "v_mfma_f32_16x16x32_bf16 v[60:63], a[24:27], a[40:43], v[60:63]\n\t"
    "ds_read_b64_tr_b16 a[14:15], v129 offset:28672\n\t"
    "v_mfma_f32_16x16x32_bf16 v[60:63], a[28:31], a[44:47], v[60:63]\n\t"
    "buffer_load_dwordx4 %[vdma1], %[sw], s43 offen offset:1024 lds\n\t"
    "s_waitcnt lgkmcnt(4)\n\t"
    "v_mfma_f32_16x16x32_bf16 v[0:3], a[0:3], a[64:67], v[0:3]\n\t"
    "ds_read_b64_tr_b16 a[16:17], v130 offset:16384\n\t"
    "buffer_load_dwordx4 a[32:35], %[vtok], %[stok], s50 offen offset:0 sc1\n\t"
    "v_mfma_f32_16x16x32_bf16 v[0:3], a[4:7], a[68:71], v[0:3]\n\t"
    "ds_read_b64_tr_b16 a[18:19], v130 offset:20480\n\t"
    "v_mfma_f32_16x16x32_bf16 v[4:7], a[0:3], a[72:75], v[4:7]\n\t"
    "ds_read_b64_tr_b16 a[20:21], v130 offset:24576\n\t"
    "v_mfma_f32_16x16x32_bf16 v[4:7], a[4:7], a[76:79], v[4:7]\n\t"
    "ds_read_b64_tr_b16 a[22:23], v130 offset:28672\n\t"
    "s_waitcnt lgkmcnt(4)\n\t"
    "v_mfma_f32_16x16x32_bf16 v[8:11], a[8:11], a[64:67], v[8:11]\n\t"
    "ds_read_b64_tr_b16 a[24:25], v131 offset:16384\n\t"
    "buffer_load_dwordx4 %[vdma2], %[sw], s43 offen offset:2048 lds\n\t"
    "v_mfma_f32_16x16x32_bf16 v[8:11], a[12:15], a[68:71], v[8:11]\n\t"
    "ds_read_b64_tr_b16 a[26:27], v131 offset:20480\n\t"
    "v_mfma_f32_16x16x32_bf16 v[12:15], a[8:11], a[72:75], v[12:15]\n\t"
    "ds_read_b64_tr_b16 a[28:29], v131 offset:24576\n\t"
    "v_mfma_f32_16x16x32_bf16 v[12:15], a[12:15], a[76:79], v[12:15]\n\t"
    "ds_read_b64_tr_b16 a[30:31], v131 offset:28672\n\t"
    "s_waitcnt lgkmcnt(4)\n\t"
    "v_mfma_f32_16x16x32_bf16 v[16:19], a[16:19], a[64:67], v[16:19]\n\t"
    "ds_read_b64_tr_b16 a[0:1], v132 offset:16384\n\t"
    "buffer_load_dwordx4 a[36:39], %[vtok], %[stok], s51 offen offset:0 sc1\n\t"
    "v_mfma_f32_16x16x32_bf16 v[16:19], a[20:23], a[68:71], v[16:19]\n\t"
    "ds_read_b64_tr_b16 a[2:3], v132 offset:20480\n\t"
    "v_mfma_f32_16x16x32_bf16 v[20:23], a[16:19], a[72:75], v[20:23]\n\t"
    "ds_read_b64_tr_b16 a[4:5], v132 offset:24576\n\t"
    "v_mfma_f32_16x16x32_bf16 v[20:23], a[20:23], a[76:79], v[20:23]\n\t"
    "ds_read_b64_tr_b16 a[6:7], v132 offset:28672\n\t"
    "s_waitcnt lgkmcnt(4)\n\t"
    "v_mfma_f32_16x16x32_bf16 v[24:27], a[24:27], a[64:67], v[24:27]\n\t"
    "ds_read_b64_tr_b16 a[8:9], v133 offset:16384\n\t"
    "buffer_load_dwordx4 a[40:43], %[vtok], %[stok], s50 offen offset:1024 sc1\n\t"
    "v_mfma_f32_16x16x32_bf16 v[24:27], a[28:31], a[68:71], v[24:27]\n\t"
    "ds_read_b64_tr_b16 a[10:11], v133 offset:20480\n\t"
    "v_mfma_f32_16x16x32_bf16 v[28:31], a[24:27], a[72:75], v[28:31]\n\t"
    "ds_read_b64_tr_b16 a[12:13], v133 offset:24576\n\t"
    "v_mfma_f32_16x16x32_bf16 v[28:31], a[28:31], a[76:79], v[28:31]\n\t"
    "ds_read_b64_tr_b16 a[14:15], v133 offset:28672\n\t"
    "s_waitcnt lgkmcnt(4)\n\t"
    "v_mfma_f32_16x16x32_bf16 v[32:35], a[0:3], a[64:67], v[32:35]\n\t"
    "ds_read_b64_tr_b16 a[16:17], v134 offset:16384\n\t"
    "buffer_load_dwordx4 %[vdma3], %[sw], s43 offen offset:3072 lds\n\t"
    "s_add_u32 s42, s42, 131072\n\t"
    "ds_read_b64_tr_b16 a[18:19], v134 offset:20480\n\t"
    "v_mfma_f32_16x16x32_bf16 v[32:35], a[4:7], a[68:71], v[32:35]\n\t"
    "ds_read_b64_tr_b16 a[20:21], v134 offset:24576\n\t"
    "ds_read_b64_tr_b16 a[22:23], v134 offset:28672\n\t"
    "v_mfma_f32_16x16x32_bf16 v[36:39], a[0:3], a[72:75], v[36:39]\n\t"
    "ds_read_b64_tr_b16 a[24:25], v135 offset:16384\n\t"
    "ds_read_b64_tr_b16 a[26:27], v135 offset:20480\n\t"
    "v_mfma_f32_16x16x32_bf16 v[36:39], a[4:7], a[76:79], v[36:39]\n\t"
    "ds_read_b64_tr_b16 a[28:29], v135 offset:24576\n\t"
    "ds_read_b64_tr_b16 a[30:31], v135 offset:28672\n\t"
    "s_waitcnt lgkmcnt(8)\n\t"
    "v_mfma_f32_16x16x32_bf16 v[40:43], a[8:11], a[64:67], v[40:43]\n\t"
    "buffer_load_dwordx4 a[44:47], %[vtok], %[stok], s51 offen offset:1024 sc1\n\t"
    "s_add_u32 s50, s50, 262144\n\t"
    "s_add_u32 s51, s51, 262144\n\t"
    "v_mfma_f32_16x16x32_bf16 v[40:43], a[12:15], a[68:71], v[40:43]\n\t"
    "v_mfma_f32_16x16x32_bf16 v[44:47], a[8:11], a[72:75], v[44:47]\n\t"
    "v_mfma_f32_16x16x32_bf16 v[44:47], a[12:15], a[76:79], v[44:47]\n\t"
    "s_cmp_lg_u32 s45, 7\n\t"
    "s_cbranch_scc1 L_g2_w1_%=\n\t"
    "s_waitcnt vmcnt(11)\n\t"
    "L_g2_w1_%=:\n\t"
    "s_waitcnt vmcnt(16) lgkmcnt(0)\n\t"
    "s_barrier\n\t"
    "v_mfma_f32_16x16x32_bf16 v[48:51], a[16:19], a[64:67], v[48:51]\n\t"
    "ds_read_b64_tr_b16 a[0:1], v128 offset:32768\n\t"
    "ds_read_b64_tr_b16 a[2:3], v128 offset:36864\n\t"
    "v_mfma_f32_16x16x32_bf16 v[48:51], a[20:23], a[68:71], v[48:51]\n\t"
    "ds_read_b64_tr_b16 a[4:5], v128 offset:40960\n\t"
    "v_mfma_f32_16x16x32_bf16 v[52:55], a[16:19], a[72:75], v[52:55]\n\t"
    "ds_read_b64_tr_b16 a[6:7], v128 offset:45056\n\t"
    "v_mfma_f32_16x16x32_bf16 v[52:55], a[20:23], a[76:79], v[52:55]\n\t"
    "s_add_u32 m0, %[sm0], 16384\n\t"
    "s_add_u32 s43, s42, 0\n\t"
    "buffer_load_dwordx4 %[vdma0], %[sw], s43 offen offset:0 lds\n\t"
    "v_mfma_f32_16x16x32_bf16 v[56:59], a[24:27], a[64:67], v[56:59]\n\t"
    "ds_read_b64_tr_b16 a[8:9], v129 offset:32768\n\t"
    "ds_read_b64_tr_b16 a[10:11], v129 offset:36864\n\t"
    "v_mfma_f32_16x16x32_bf16 v[56:59], a[28:31], a[68:71], v[56:59]\n\t"
    "ds_read_b64_tr_b16 a[12:13], v129 offset:40960\n\t"
    "v_mfma_f32_16x16x32_bf16 v[60:63], a[24:27], a[72:75], v[60:63]\n\t"
    "ds_read_b64_tr_b16 a[14:15], v129 offset:45056\n\t"
    "v_mfma_f32_16x16x32_bf16 v[60:63], a[28:31], a[76:79], v[60:63]\n\t"
    "buffer_load_dwordx4 %[vdma1], %[sw], s43 offen offset:1024 lds\n\t"
    "s_waitcnt lgkmcnt(4)\n\t"
    "v_mfma_f32_16x16x32_bf16 v[0:3], a[0:3], a[96:99], v[0:3]\n\t"
    "ds_read_b64_tr_b16 a[16:17], v130 offset:32768\n\t"
    "buffer_load_dwordx4 a[64:67], %[vtok], %[stok], s50 offen offset:0 sc1\n\t"
    "v_mfma_f32_16x16x32_bf16 v[0:3], a[4:7], a[100:103], v[0:3]\n\t"
    "ds_read_b64_tr_b16 a[18:19], v130 offset:36864\n\t"
    "v_mfma_f32_16x16x32_bf16 v[4:7], a[0:3], a[104:107], v[4:7]\n\t"
    "ds_read_b64_tr_b16 a[20:21], v130 offset:40960\n\t"
    "v_mfma_f32_16x16x32_bf16 v[4:7], a[4:7], a[108:111], v[4:7]\n\t"
    "ds_read_b64_tr_b16 a[22:23], v130 offset:45056\n\t"
    "s_waitcnt lgkmcnt(4)\n\t"
    "v_mfma_f32_16x16x32_bf16 v[8:11], a[8:11], a[96:99], v[8:11]\n\t"
    "ds_read_b64_tr_b16 a[24:25], v131 offset:32768\n\t"
    "buffer_load_dwordx4 %[vdma2], %[sw], s43 offen offset:2048 lds\n\t"
    "v_mfma_f32_16x16x32_bf16 v[8:11], a[12:15], a[100:103], v[8:11]\n\t"
    "ds_read_b64_tr_b16 a[26:27], v131 offset:36864\n\t"
    "v_mfma_f32_16x16x32_bf16 v[12:15], a[8:11], a[104:107], v[12:15]\n\t"
    "ds_read_b64_tr_b16 a[28:29], v131 offset:40960\n\t"
    "v_mfma_f32_16x16x32_bf16 v[12:15], a[12:15], a[108:111], v[12:15]\n\t"
    "ds_read_b64_tr_b16 a[30:31], v131 offset:45056\n\t"
    "s_waitcnt lgkmcnt(4)\n\t"
    "v_mfma_f32_16x16x32_bf16 v[16:19], a[16:19], a[96:99], v[16:19]\n\t"
    "ds_read_b64_tr_b16 a[0:1], v132 offset:32768\n\t"
    "buffer_load_dwordx4 a[68:71], %[vtok], %[stok], s51 offen offset:0 sc1\n\t"
    "v_mfma_f32_16x16x32_bf16 v[16:19], a[20:23], a[100:103], v[16:19]\n\t"
    "ds_read_b64_tr_b16 a[2:3], v132 offset:36864\n\t"
    "v_mfma_f32_16x16x32_bf16 v[20:23], a[16:19], a[104:107], v[20:23]\n\t"
    "ds_read_b64_tr_b16 a[4:5], v132 offset:40960\n\t"
    "v_mfma_f32_16x16x32_bf16 v[20:23], a[20:23], a[108:111], v[20:23]\n\t"
    "ds_read_b64_tr_b16 a[6:7], v132 offset:45056\n\t"
    "s_waitcnt lgkmcnt(4)\n\t"
    "v_mfma_f32_16x16x32_bf16 v[24:27], a[24:27], a[96:99], v[24:27]\n\t"
    "ds_read_b64_tr_b16 a[8:9], v133 offset:32768\n\t"
    "buffer_load_dwordx4 a[72:75], %[vtok], %[stok], s50 offen offset:1024 sc1\n\t"
    "v_mfma_f32_16x16x32_bf16 v[24:27], a[28:31], a[100:103], v[24:27]\n\t"
    "ds_read_b64_tr_b16 a[10:11], v133 offset:36864\n\t"
    "v_mfma_f32_16x16x32_bf16 v[28:31], a[24:27], a[104:107], v[28:31]\n\t"
    "ds_read_b64_tr_b16 a[12:13], v133 offset:40960\n\t"
    "v_mfma_f32_16x16x32_bf16 v[28:31], a[28:31], a[108:111], v[28:31]\n\t"
    "ds_read_b64_tr_b16 a[14:15], v133 offset:45056\n\t"
    "s_waitcnt lgkmcnt(4)\n\t"
    "v_mfma_f32_16x16x32_bf16 v[32:35], a[0:3], a[96:99], v[32:35]\n\t"
    "ds_read_b64_tr_b16 a[16:17], v134 offset:32768\n\t"
    "buffer_load_dwordx4 %[vdma3], %[sw], s43 offen offset:3072 lds\n\t"
    "s_add_u32 s42, s42, 131072\n\t"
    "ds_read_b64_tr_b16 a[18:19], v134 offset:36864\n\t"
    "v_mfma_f32_16x16x32_bf16 v[32:35], a[4:7], a[100:103], v[32:35]\n\t"
    "ds_read_b64_tr_b16 a[20:21], v134 offset:40960\n\t"
    "ds_read_b64_tr_b16 a[22:23], v134 offset:45056\n\t"
    "v_mfma_f32_16x16x32_bf16 v[36:39], a[0:3], a[104:107], v[36:39]\n\t"
    "ds_read_b64_tr_b16 a[24:25], v135 offset:32768\n\t"
    "ds_read_b64_tr_b16 a[26:27], v135 offset:36864\n\t"
    "v_mfma_f32_16x16x32_bf16 v[36:39], a[4:7], a[108:111], v[36:39]\n\t"
    "ds_read_b64_tr_b16 a[28:29], v135 offset:40960\n\t"
    "ds_read_b64_tr_b16 a[30:31], v135 offset:45056\n\t"
    "s_waitcnt lgkmcnt(8)\n\t"
    "v_mfma_f32_16x16x32_bf16 v[40:43], a[8:11], a[96:99], v[40:43]\n\t"
    "buffer_load_dwordx4 a[76:79], %[vtok], %[stok], s51 offen offset:1024 sc1\n\t"
    "s_add_u32 s50, s50, 262144\n\t"
    "s_add_u32 s51, s51, 262144\n\t"
    "v_mfma_f32_16x16x32_bf16 v[40:43], a[12:15], a[100:103], v[40:43]\n\t"
    "v_mfma_f32_16x16x32_bf16 v[44:47], a[8:11], a[104:107], v[44:47]\n\t"
    "v_mfma_f32_16x16x32_bf16 v[44:47], a[12:15], a[108:111], v[44:47]\n\t"
    "s_waitcnt vmcnt(16) lgkmcnt(0)\n\t"
    "s_barrier\n\t"
    "v_mfma_f32_16x16x32_bf16 v[48:51], a[16:19], a[96:99], v[48:51]\n\t"
    "ds_read_b64_tr_b16 a[0:1], v128 offset:49152\n\t"
    "ds_read_b64_tr_b16 a[2:3], v128 offset:53248\n\t"
    "v_mfma_f32_16x16x32_bf16 v[48:51], a[20:23], a[100:103], v[48:51]\n\t"
    "ds_read_b64_tr_b16 a[4:5], v128 offset:57344\n\t"
    "v_mfma_f32_16x16x32_bf16 v[52:55], a[16:19], a[104:107], v[52:55]\n\t"
    "ds_read_b64_tr_b16 a[6:7], v128 offset:61440\n\t"
    "v_mfma_f32_16x16x32_bf16 v[52:55], a[20:23], a[108:111], v[52:55]\n\t"
    "s_add_u32 m0, %[sm0], 32768\n\t"
    "s_add_u32 s43, s42, 0\n\t"
    "buffer_load_dwordx4 %[vdma0], %[sw], s43 offen offset:0 lds\n\t"
    "v_mfma_f32_16x16x32_bf16 v[56:59], a[24:27], a[96:99], v[56:59]\n\t"
    "ds_read_b64_tr_b16 a[8:9], v129 offset:49152\n\t"
    "ds_read_b64_tr_b16 a[10:11], v129 offset:53248\n\t"
    "v_mfma_f32_16x16x32_bf16 v[56:59], a[28:31], a[100:103], v[56:59]\n\t"
    "ds_read_b64_tr_b16 a[12:13], v129 offset:57344\n\t"
    "v_mfma_f32_16x16x32_bf16 v[60:63], a[24:27], a[104:107], v[60:63]\n\t"
    "ds_read_b64_tr_b16 a[14:15], v129 offset:61440\n\t"
    "v_mfma_f32_16x16x32_bf16 v[60:63], a[28:31], a[108:111], v[60:63]\n\t"
    "buffer_load_dwordx4 %[vdma1], %[sw], s43 offen offset:1024 lds\n\t"
    "s_waitcnt lgkmcnt(4)\n\t"
    "v_mfma_f32_16x16x32_bf16 v[0:3], a[0:3], a[128:131], v[0:3]\n\t"
    "ds_read_b64_tr_b16 a[16:17], v130 offset:49152\n\t"
    "buffer_load_dwordx4 a[96:99], %[vtok], %[stok], s50 offen offset:0 sc1\n\t"
    "v_mfma_f32_16x16x32_bf16 v[0:3], a[4:7], a[132:135], v[0:3]\n\t"
    "ds_read_b64_tr_b16 a[18:19], v130 offset:53248\n\t"
    "v_mfma_f32_16x16x32_bf16 v[4:7], a[0:3], a[136:139], v[4:7]\n\t"
    "ds_read_b64_tr_b16 a[20:21], v130 offset:57344\n\t"
    "v_mfma_f32_16x16x32_bf16 v[4:7], a[4:7], a[140:143], v[4:7]\n\t"
    "ds_read_b64_tr_b16 a[22:23], v130 offset:61440\n\t"
    "s_waitcnt lgkmcnt(4)\n\t"
    "v_mfma_f32_16x16x32_bf16 v[8:11], a[8:11], a[128:131], v[8:11]\n\t"
    "ds_read_b64_tr_b16 a[24:25], v131 offset:49152\n\t"
    "buffer_load_dwordx4 %[vdma2], %[sw], s43 offen offset:2048 lds\n\t"
    "v_mfma_f32_16x16x32_bf16 v[8:11], a[12:15], a[132:135], v[8:11]\n\t"
    "ds_read_b64_tr_b16 a[26:27], v131 offset:53248\n\t"
    "v_mfma_f32_16x16x32_bf16 v[12:15], a[8:11], a[136:139], v[12:15]\n\t"
    "ds_read_b64_tr_b16 a[28:29], v131 offset:57344\n\t"
    "v_mfma_f32_16x16x32_bf16 v[12:15], a[12:15], a[140:143], v[12:15]\n\t"
    "ds_read_b64_tr_b16 a[30:31], v131 offset:61440\n\t"
    "s_waitcnt lgkmcnt(4)\n\t"
    "v_mfma_f32_16x16x32_bf16 v[16:19], a[16:19], a[128:131], v[16:19]\n\t"
    "ds_read_b64_tr_b16 a[0:1], v132 offset:49152\n\t"
    "buffer_load_dwordx4 a[100:103], %[vtok], %[stok], s51 offen offset:0 sc1\n\t"
    "v_mfma_f32_16x16x32_bf16 v[16:19], a[20:23], a[132:135], v[16:19]\n\t"
    "ds_read_b64_tr_b16 a[2:3], v132 offset:53248\n\t"
    "v_mfma_f32_16x16x32_bf16 v[20:23], a[16:19], a[136:139], v[20:23]\n\t"
    "ds_read_b64_tr_b16 a[4:5], v132 offset:57344\n\t"
    "v_mfma_f32_16x16x32_bf16 v[20:23], a[20:23], a[140:143], v[20:23]\n\t"
    "ds_read_b64_tr_b16 a[6:7], v132 offset:61440\n\t"
    "s_waitcnt lgkmcnt(4)\n\t"
    "v_mfma_f32_16x16x32_bf16 v[24:27], a[24:27], a[128:131], v[24:27]\n\t"
    "ds_read_b64_tr_b16 a[8:9], v133 offset:49152\n\t"
    "buffer_load_dwordx4 a[104:107], %[vtok], %[stok], s50 offen offset:1024 sc1\n\t"
    "v_mfma_f32_16x16x32_bf16 v[24:27], a[28:31], a[132:135], v[24:27]\n\t"
    "ds_read_b64_tr_b16 a[10:11], v133 offset:53248\n\t"
    "v_mfma_f32_16x16x32_bf16 v[28:31], a[24:27], a[136:139], v[28:31]\n\t"
    "ds_read_b64_tr_b16 a[12:13], v133 offset:57344\n\t"
    "v_mfma_f32_16x16x32_bf16 v[28:31], a[28:31], a[140:143], v[28:31]\n\t"
    "ds_read_b64_tr_b16 a[14:15], v133 offset:61440\n\t"
    "s_waitcnt lgkmcnt(4)\n\t"
    "v_mfma_f32_16x16x32_bf16 v[32:35], a[0:3], a[128:131], v[32:35]\n\t"
    "ds_read_b64_tr_b16 a[16:17], v134 offset:49152\n\t"
    "buffer_load_dwordx4 %[vdma3], %[sw], s43 offen offset:3072 lds\n\t"
    "s_add_u32 s42, s42, 131072\n\t"
    "ds_read_b64_tr_b16 a[18:19], v134 offset:53248\n\t"
    "v_mfma_f32_16x16x32_bf16 v[32:35], a[4:7], a[132:135], v[32:35]\n\t"
    "ds_read_b64_tr_b16 a[20:21], v134 offset:57344\n\t"
    "ds_read_b64_tr_b16 a[22:23], v134 offset:61440\n\t"
    "v_mfma_f32_16x16x32_bf16 v[36:39], a[0:3], a[136:139], v[36:39]\n\t"
    "ds_read_b64_tr_b16 a[24:25], v135 offset:49152\n\t"
    "ds_read_b64_tr_b16 a[26:27], v135 offset:53248\n\t"
    "v_mfma_f32_16x16x32_bf16 v[36:39], a[4:7], a[140:143], v[36:39]\n\t"
    "ds_read_b64_tr_b16 a[28:29], v135 offset:57344\n\t"
    "ds_read_b64_tr_b16 a[30:31], v135 offset:61440\n\t"
    "s_waitcnt lgkmcnt(8)\n\t"
    "v_mfma_f32_16x16x32_bf16 v[40:43], a[8:11], a[128:131], v[40:43]\n\t"
    "buffer_load_dwordx4 a[108:111], %[vtok], %[stok], s51 offen offset:1024 sc1\n\t"
    "s_add_u32 s50, s50, 262144\n\t"
    "s_add_u32 s51, s51, 262144\n\t"
    "v_mfma_f32_16x16x32_bf16 v[40:43], a[12:15], a[132:135], v[40:43]\n\t"
    "v_mfma_f32_16x16x32_bf16 v[44:47], a[8:11], a[136:139], v[44:47]\n\t"
    "v_mfma_f32_16x16x32_bf16 v[44:47], a[12:15], a[140:143], v[44:47]\n\t"
    "s_waitcnt vmcnt(16) lgkmcnt(0)\n\t"
    "s_barrier\n\t"
    "v_mfma_f32_16x16x32_bf16 v[48:51], a[16:19], a[128:131], v[48:51]\n\t"
    "ds_read_b64_tr_b16 a[0:1], v128 offset:0\n\t"
    "ds_read_b64_tr_b16 a[2:3], v128 offset:4096\n\t"
    "v_mfma_f32_16x16x32_bf16 v[48:51], a[20:23], a[132:135], v[48:51]\n\t"
    "ds_read_b64_tr_b16 a[4:5], v128 offset:8192\n\t"
    "v_mfma_f32_16x16x32_bf16 v[52:55], a[16:19], a[136:139], v[52:55]\n\t"
    "ds_read_b64_tr_b16 a[6:7], v128 offset:12288\n\t"
    "v_mfma_f32_16x16x32_bf16 v[52:55], a[20:23], a[140:143], v[52:55]\n\t"
    "s_add_u32 m0, %[sm0], 49152\n\t"
    "s_add_u32 s43, s42, 0\n\t"
    "buffer_load_dwordx4 %[vdma0], %[sw], s43 offen offset:0 lds\n\t"
    "v_mfma_f32_16x16x32_bf16 v[56:59], a[24:27], a[128:131], v[56:59]\n\t"
    "ds_read_b64_tr_b16 a[8:9], v129 offset:0\n\t"
    "ds_read_b64_tr_b16 a[10:11], v129 offset:4096\n\t"
    "v_mfma_f32_16x16x32_bf16 v[56:59], a[28:31], a[132:135], v[56:59]\n\t"
    "ds_read_b64_tr_b16 a[12:13], v129 offset:8192\n\t"
    "v_mfma_f32_16x16x32_bf16 v[60:63], a[24:27], a[136:139], v[60:63]\n\t"
    "ds_read_b64_tr_b16 a[14:15], v129 offset:12288\n\t"
    "v_mfma_f32_16x16x32_bf16 v[60:63], a[28:31], a[140:143], v[60:63]\n\t"
    "buffer_load_dwordx4 %[vdma1], %[sw], s43 offen offset:1024 lds\n\t"
    "s_sub_u32 s45, s45, 1\n\t"
    "s_cmp_lg_u32 s45, 0\n\t"
    "s_cbranch_scc1 L_g2_loop_%=\n\t"
    "L_g2_done_%=:\n\t"
    "s_waitcnt vmcnt(0) lgkmcnt(0)\n\t"
    "s_barrier\n\t"
    "s_nop 7\n\t"
      : "=&{v[0:15]}"(a0), "=&{v[16:31]}"(a1), "=&{v[32:47]}"(a2), "=&{v[48:63]}"(a3),
        [vt] "=&v"(vt), [vt2] "=&v"(vt2)
      : [vtrA] "v"(vtrA), [vtok] "v"(vtok), [vdma0] "v"(vdma0), [vdma1] "v"(vdma1), [vdma2] "v"(vdma2), [vdma3] "v"(vdma3), [stok] "s"(stok), [sw] "s"(sw), [sm0] "s"(sm0), [flag] "s"(flag), [want] "s"(want), [wid] "s"(w), [vpoll] "v"(vpoll)
      : "memory", "scc", "vcc", "a0", "a1", "a2", "a3", "a4", "a5", "a6", "a7", "a8", "a9", "a10", "a11", "a12", "a13", "a14", "a15", "a16", "a17", "a18", "a19", "a20", "a21", "a22", "a23", "a24", "a25", "a26", "a27", "a28", "a29", "a30", "a31", "a32", "a33", "a34", "a35", "a36", "a37", "a38", "a39", "a40", "a41", "a42", "a43", "a44", "a45", "a46", "a47", "a48", "a49", "a50", "a51", "a52", "a53", "a54", "a55", "a56", "a57", "a58", "a59", "a60", "a61", "a62", "a63", "a64", "a65", "a66", "a67", "a68", "a69", "a70", "a71", "a72", "a73", "a74", "a75", "a76", "a77", "a78", "a79", "a80", "a81", "a82", "a83", "a84", "a85", "a86", "a87", "a88", "a89", "a90", "a91", "a92", "a93", "a94", "a95", "a96", "a97", "a98", "a99", "a100", "a101", "a102", "a103", "a104", "a105", "a106", "a107", "a108", "a109", "a110", "a111", "a112", "a113", "a114", "a115", "a116", "a117", "a118", "a119", "a120", "a121", "a122", "a123", "a124", "a125", "a126", "a127", "a128", "a129", "a130", "a131", "a132", "a133", "a134", "a135", "a136", "a137", "a138", "a139", "a140", "a141", "a142", "a143", "a144", "a145", "a146", "a147", "a148", "a149", "a150", "a151", "a152", "a153", "a154", "a155", "a156", "a157", "a158", "a159", "s42", "s43", "s44", "s45", "s46", "s47", "s48", "s49", "s50", "s51", "s52", "s53", "s54", "s55", "s56", "s57", "s58", "s59", "s60", "s61", "v128", "v129", "v130", "v131", "v132", "v133", "v134", "v135", "v136", "v137", "v138", "v139", "v140", "v141", "v142", "v143");
    const int tile = pm * 8 + pn;
    const bool recv = (w >> 1) == kh;
    const int row0 = pm * 128 + w * 32, col0 = pn * 128 + 16 * (g & 1) + 8 * (g >> 1);
    const int slot = (tile * 2 + (w >> 1)) * 2 + (w & 1);
    if (!recv) {
      char* dst = (char*)P.xch + (size_t)slot * 8192 + lane * 16;
      auto send = [&](const f32x16& s, int j) {
#pragma unroll
        for (int m = 0; m < 2; m++) {
          auto x0 = __builtin_amdgcn_permlane16_swap(pk2(s[4 * m], s[4 * m + 1]), pk2(s[8 + 4 * m], s[8 + 4 * m + 1]), false, false);
          auto x1 = __builtin_amdgcn_permlane16_swap(pk2(s[4 * m + 2], s[4 * m + 3]), pk2(s[8 + 4 * m + 2], s[8 + 4 * m + 3]), false, false);
          asm volatile("global_store_dwordx4 %0, %1, off sc1\n\ts_nop 1" :: "v"(dst + (2 * j + m) * 1024), "v"((u32x4){x0[0], x1[0], x0[1], x1[1]}) : "memory");
        }
      };
      send(a0, 0); send(a1, 1); send(a2, 2); send(a3, 3);
      asm volatile("s_waitcnt vmcnt(0)" ::: "memory");
      if (lane == 0) __hip_atomic_store((gu32*)(P.flags + FL_X + slot * 16), 1u, __ATOMIC_RELAXED, __HIP_MEMORY_SCOPE_AGENT);
    } else {
      f32x4 oa[8], ob2[8];
      auto keep = [&](const f32x16& s, int j) {
#pragma unroll
        for (int m = 0; m < 2; m++)
#pragma unroll
          for (int e = 0; e < 4; e++) {
            auto x = __builtin_amdgcn_permlane16_swap(__float_as_uint(s[4 * m + e]), __float_as_uint(s[8 + 4 * m + e]), false, false);
            oa[2 * j + m][e] = __uint_as_float(x[0]); ob2[2 * j + m][e] = __uint_as_float(x[1]);
          }
      };
      keep(a0, 0); keep(a1, 1); keep(a2, 2); keep(a3, 3);
      u32x4 hv[8];
      {
        const char* hp = (const char*)(P.h + (size_t)(row0 + (lane & 15)) * D_ + col0);
        asm volatile("global_load_dwordx4 %0, %8, off sc1\n\tglobal_load_dwordx4 %1, %9, off sc1\n\tglobal_load_dwordx4 %2, %8, off offset:64 sc1\n\tglobal_load_dwordx4 %3, %9, off offset:64 sc1\n\t"
                     "global_load_dwordx4 %4, %8, off offset:128 sc1\n\tglobal_load_dwordx4 %5, %9, off offset:128 sc1\n\tglobal_load_dwordx4 %6, %8, off offset:192 sc1\n\tglobal_load_dwordx4 %7, %9, off offset:192 sc1\n\ts_waitcnt vmcnt(0)"
                     : "=&v"(hv[0]), "=&v"(hv[1]), "=&v"(hv[2]), "=&v"(hv[3]), "=&v"(hv[4]), "=&v"(hv[5]), "=&v"(hv[6]), "=&v"(hv[7])
                     : "v"(hp), "v"(hp + 32768) : "memory");
      }
      gu32* fl = (gu32*)(P.flags + FL_X + slot * 16);
      u32 spins = 0;
#pragma unroll 1
      while (rfl(__hip_atomic_load(fl, __ATOMIC_RELAXED, __HIP_MEMORY_SCOPE_AGENT)) == 0u) { __builtin_amdgcn_s_sleep(1); if (++spins > (1u << 17)) break; }
      const char* src = (const char*)P.xch + (size_t)slot * 8192 + lane * 16;
      u32x4 pr[8];
      asm volatile(
        "global_load_dwordx4 %0, %8, off sc1\n\tglobal_load_dwordx4 %1, %8, off offset:1024 sc1\n\tglobal_load_dwordx4 %2, %8, off offset:2048 sc1\n\tglobal_load_dwordx4 %3, %8, off offset:3072 sc1\n\t"
        "global_load_dwordx4 %4, %9, off sc1\n\tglobal_load_dwordx4 %5, %9, off offset:1024 sc1\n\tglobal_load_dwordx4 %6, %9, off offset:2048 sc1\n\tglobal_load_dwordx4 %7, %9, off offset:3072 sc1\n\t"
        "s_waitcnt vmcnt(0)"
        : "=&v"(pr[0]), "=&v"(pr[1]), "=&v"(pr[2]), "=&v"(pr[3]), "=&v"(pr[4]), "=&v"(pr[5]), "=&v"(pr[6]), "=&v"(pr[7])
        : "v"(src), "v"(src + 4096) : "memory");
      char* op = (char*)(P.out + (size_t)(row0 + (lane & 15)) * D_ + col0);
#pragma unroll
      for (int i = 0; i < 8; i++) {
        const u32x4 o = {pk2(oa[i][0] + bflo(pr[i][0]), oa[i][1] + bfhi(pr[i][0])), pk2(oa[i][2] + bflo(pr[i][1]), oa[i][3] + bfhi(pr[i][1])),
                         pk2(ob2[i][0] + bflo(pr[i][2]), ob2[i][1] + bfhi(pr[i][2])), pk2(ob2[i][2] + bflo(pr[i][3]), ob2[i][3] + bfhi(pr[i][3]))};
        const u32x4 hh = hv[i];
        u32x4 res;
#pragma unroll
        for (int e = 0; e < 4; e++) res[e] = pk2(bflo(hh[e]) + bflo(o[e]), bfhi(hh[e]) + bfhi(o[e]));
        asm volatile("global_store_dwordx4 %0, %1, off sc1 nt\n\ts_nop 1" :: "v"(op + (size_t)(i & 1) * 32768 + (i >> 1) * 64), "v"(res) : "memory");
      }
    }
  }
}

__global__ __launch_bounds__(512) void k_kv(const u16* __restrict__ k, const u16* __restrict__ v, const u16* __restrict__ sk, u16* __restrict__ kvB, u32* __restrict__ flags, const u16* __restrict__ f) {
  const int u = blockIdx.x, t = threadIdx.x;
  if (t < 64) flags[u * 64 + t] = 0u;
  u32 kk[11], vv[8];
#pragma unroll
  for (int i = 0; i < 11; i++) {
    const int row = u * 8 - 3 + i;
    kk[i] = row >= 0 ? ldnt<u32>(k + (size_t)row * D_ + 2 * t) : 0u;
  }
#pragma unroll
  for (int i = 0; i < 8; i++) vv[i] = ldnt<u32>(v + (size_t)(u * 8 + i) * D_ + 2 * t);
  const u32x4 sv = *(const u32x4*)(sk + 8 * t);
  const char* fp = (const char*)f + (size_t)((u & 7) * 32 + (u >> 3)) * 16384 + t * 16;
  const u32x4 fpf0 = *(const u32x4*)fp, fpf1 = *(const u32x4*)(fp + 8192);
  u32x4 cpf = {0u, 0u, 0u, 0u};
  { const int o = ((u >> 3) * 128 + t) * 16; if (t < 128 && o < CODE_BYTES) cpf = *(const u32x4*)(main_code() + o); }
  const f32x2 w0 = {bflo(sv[0]), bflo(sv[2])}, w1 = {bfhi(sv[0]), bfhi(sv[2])}, w2 = {bflo(sv[1]), bflo(sv[3])}, w3 = {bfhi(sv[1]), bfhi(sv[3])};
  f32x2 kf[11], o[8];
#pragma unroll
  for (int i = 0; i < 11; i++) kf[i] = (f32x2){bflo(kk[i]), bfhi(kk[i])};
#pragma unroll
  for (int r = 0; r < 8; r++) {
    f32x2 a = kf[r] * w3;
    a = kf[r + 1] * w2 + a; a = kf[r + 2] * w1 + a; a = kf[r + 3] * w0 + a;
    const u32 ar = pk2(a[0], a[1]);
    o[r] = (f32x2){bflo(ar), bfhi(ar)} * (f32x2){bflo(vv[r]), bfhi(vv[r])};
  }
  const u32x4 c0 = {pk2(o[7][0], o[6][0]), pk2(o[5][0], o[4][0]), pk2(o[3][0], o[2][0]), pk2(o[1][0], o[0][0])};
  const u32x4 c1 = {pk2(o[7][1], o[6][1]), pk2(o[5][1], o[4][1]), pk2(o[3][1], o[2][1]), pk2(o[1][1], o[0][1])};
  char* dst = (char*)kvB + ((size_t)u * D_ + 2 * t) * 16;
  *(u32x4*)dst = c0; *(u32x4*)(dst + 16) = c1;
  asm volatile("" :: "v"(cpf), "v"(fpf0), "v"(fpf1));
}

extern "C" void kernel_launch(void* const* d_in, const int* in_sizes, int n_in, void* d_out, int out_size, void* d_ws, size_t ws_size, hipStream_t stream) {
  const u16 *k = (const u16*)d_in[1], *v = (const u16*)d_in[2], *sk = (const u16*)d_in[3];
  char* ws = (char*)d_ws;
  u16* kvB = (u16*)ws;
  u32* flags = (u32*)(ws + (48u << 20));
  Params P;
  P.x = (const u16*)d_in[0]; P.f = (const u16*)d_in[4]; P.sD = (const u16*)d_in[5]; P.kvB = kvB;
  P.wg = (const u16*)d_in[7]; P.wu = (const u16*)d_in[8]; P.wd = (const u16*)d_in[9];
  P.zB = (u16*)(ws + (4u << 20));
  P.h = (u16*)(ws + (8u << 20)); P.hn = (u16*)(ws + (12u << 20)); P.hm = (u16*)(ws + (16u << 20)); P.out = (u16*)d_out;
  P.xch = (float*)(ws + (32u << 20));
  P.flags = flags;
  (void)hipFuncSetAttribute((const void*)k_main, hipFuncAttributeMaxDynamicSharedMemorySize, GM_LDS);
  k_kv<<<256, 512, 0, stream>>>(k, v, sk, kvB, flags, P.f);
  k_main<<<256, 256, GM_LDS, stream>>>(P);
}
```
